```python
import torch

def _softmax(x, axis=-1):
    return torch.softmax(x, dim=axis)
def _gelu(x):
    return torch.nn.functional.gelu(x, approximate="tanh")
def _silu(x):
    return torch.nn.functional.silu(x)
def _sigmoid(x):
    return torch.sigmoid(x)
def _layernorm(x, axis=-1, eps=1e-5):
    m = x.mean(dim=axis, keepdim=True)
    v = x.var(dim=axis, keepdim=True, unbiased=False)
    return (x - m) / torch.sqrt(v + eps)
def _scatter_add(tgt, idx, src, axis=0):
    return tgt.clone().index_add_(axis, idx, src)
def _segment_sum(src, seg, n):
    out = torch.zeros((n,) + src.shape[1:], dtype=src.dtype)
    return out.index_add_(0, seg, src)
def _pad(x, pw):
    flat = tuple(v for pair in reversed(pw) for v in pair)
    return torch.nn.functional.pad(x, flat)

kernel_name = 'parallel_sum__sliding_local_dilated__geglu_ffn__prompt'

def setup_inputs():

    _w = (lambda a: (lambda t: t.to(torch.bfloat16) if t.is_floating_point() else t)(torch.as_tensor(a)))
    def _A_setup():
        B, S, D = 1, 2048, 640
        return {
            "x": _w((lambda *s: torch.randn(*s, dtype=torch.bfloat16) * 0.05)(B, S, D)),
            "wqkv": _w((lambda *s: torch.randn(*s, dtype=torch.bfloat16) * 0.05)(D, 3 * D)),
            "wo": _w((lambda *s: torch.randn(*s, dtype=torch.bfloat16) * 0.05)(D, D)),
        }
    _A_in = _A_setup()

    def _B_setup():
        B, S, D, F = 1, 2048, 640, 1728
        return {
            "x": _w((lambda *s: torch.randn(*s, dtype=torch.bfloat16) * 0.05)(B, S, D)),
            "w_gate": _w((lambda *s: torch.randn(*s, dtype=torch.bfloat16) * 0.05)(D, F)),
            "w_up": _w((lambda *s: torch.randn(*s, dtype=torch.bfloat16) * 0.05)(D, F)),
            "w_down": _w((lambda *s: torch.randn(*s, dtype=torch.bfloat16) * 0.05)(F, D)),
        }
    _B_in = _B_setup()

    return {
        "A_x": _A_in["x"],
        "A_wqkv": _A_in["wqkv"],
        "A_wo": _A_in["wo"],
        "B_x": _B_in["x"],
        "B_w_gate": _B_in["w_gate"],
        "B_w_up": _B_in["w_up"],
        "B_w_down": _B_in["w_down"],
    }

def reference(A_x, A_wqkv, A_wo, B_x, B_w_gate, B_w_up, B_w_down):
    def _A_ref(x, wqkv, wo):
        B, S, D = x.shape
        H = 32; Dh = D // H
        W, dil = 8, 2
        qkv = (lambda x, *d: x.permute(*d))((x @ wqkv).reshape(B, S, 3, H, Dh), 2, 0, 3, 1, 4)
        q, k, v = qkv[0], qkv[1], qkv[2]
        scale = Dh ** -0.5
        pos = torch.arange(S)
        off = torch.arange(W) * dil
        idx = pos[:, None] - off[None, :]
        mask = idx >= 0
        idx_c = torch.where(mask, idx, 0).reshape(-1)
        kg = k[:, :, idx_c, :].reshape(B, H, S, W, Dh)
        vg = v[:, :, idx_c, :].reshape(B, H, S, W, Dh)
        logits = torch.einsum("bhsd,bhswd->bhsw", q, kg) * scale
        logits = torch.where(mask[None, None, :, :], logits, -1e9)
        attn = _softmax(logits, axis=-1)
        o = torch.einsum("bhsw,bhswd->bhsd", attn, vg)
        return (lambda x, *d: x.permute(*d))(o, 0, 2, 1, 3).reshape(B, S, D) @ wo

    def _B_ref(x, w_gate, w_up, w_down):
        return (_gelu(x @ w_gate) * (x @ w_up)) @ w_down

    x = A_x
    xn = _layernorm(x, axis=-1)
    return x + _A_ref(xn, A_wqkv, A_wo) + _B_ref(xn, B_w_gate, B_w_up, B_w_down)
```

```cpp
#include <hip/hip_runtime.h>
#include <hip/hip_bf16.h>
#include <stdint.h>

typedef __attribute__((ext_vector_type(8))) short bf16x8;
typedef __attribute__((ext_vector_type(4))) short s16x4;
typedef __attribute__((ext_vector_type(4))) float f32x4;
typedef __attribute__((ext_vector_type(2))) float f32x2;
typedef __attribute__((ext_vector_type(2))) __bf16 bf2;
typedef __attribute__((ext_vector_type(4))) unsigned u32x4;
typedef __attribute__((ext_vector_type(2))) unsigned u32x2;
#define LDS_AS __attribute__((address_space(3)))

constexpr int S_ = 2048, D_ = 640, F_ = 1728;
constexpr int KT2 = 74;
constexpr int NS = 20;
constexpr int WSLOT = 16384, XSLOT = 24576;
constexpr int W_OFF = 0, X_OFF = 5 * WSLOT;
constexpr int LDS1 = 5 * WSLOT + 3 * XSLOT;
constexpr int QROW = 488;
constexpr int CS_OFF = 192 * QROW;

__device__ __forceinline__ float dot2(unsigned a, unsigned b, float c) {
  return __builtin_amdgcn_fdot2_f32_bf16(__builtin_bit_cast(bf2, a), __builtin_bit_cast(bf2, b), c, false);
}
__device__ __forceinline__ unsigned pack2(float a, float b) {
  f32x2 p = {a, b};
  return __builtin_bit_cast(unsigned, __builtin_convertvector(p, bf2));
}
__device__ __forceinline__ void glds16(const char* base, unsigned voff, unsigned lds_dst) {
  unsigned keep;
  asm volatile("s_mov_b32 %0, m0\n\ts_mov_b32 m0, %3\n\ts_nop 0\n\tglobal_load_lds_dwordx4 %1, %2\n\ts_mov_b32 m0, %0"
               : "=&s"(keep) : "v"(voff), "s"(base), "s"(lds_dst) : "memory");
}
__device__ __forceinline__ s16x4 tr_read(unsigned lds_off) {
  return __builtin_amdgcn_ds_read_tr16_b64_v4i16((LDS_AS s16x4*)(uintptr_t)lds_off);
}
__device__ __forceinline__ bf16x8 lds_read16(unsigned lds_off) { return *(LDS_AS bf16x8*)(uintptr_t)lds_off; }
__device__ __forceinline__ bf16x8 cat(s16x4 a, s16x4 b) { return __builtin_shufflevector(a, b, 0, 1, 2, 3, 4, 5, 6, 7); }

constexpr int outstanding_after(int upto_step, int needW, int needX) {
  int cnt = 0;
  int pw[8] = {-1, 0, 1, -1, 2, 3, -1, 4};
  int px[8] = {0, -1, -1, 1, -1, -1, 2, -1};
  for (int i = 0; i < 8; ++i) {
    if (pw[i] >= 0) { cnt += 2; if (pw[i] == needW) cnt = 0; }
    else { cnt += 3; if (px[i] == needX) cnt = 0; }
  }
  for (int s = 0; s <= upto_step; ++s) {
    if (s + 5 < NS) { cnt += 2; if (s + 5 == needW) cnt = 0; }
    if ((s & 1) && (s + 5) / 2 < NS / 2) { cnt += 3; if ((s + 5) / 2 == needX) cnt = 0; }
  }
  return cnt;
}
template <int N> __device__ __forceinline__ void wait_vm() { asm volatile("s_waitcnt vmcnt(%0)" ::"n"(N) : "memory"); }
__device__ __forceinline__ void wait_lgkm0() { asm volatile("s_waitcnt lgkmcnt(0)" ::: "memory"); }
__device__ __forceinline__ void bar() { __builtin_amdgcn_s_barrier(); }

struct K1State {
  f32x4 acc[3][8];
  bf16x8 fw[8];
  float sx[3], sq[3], cs[2];
  const char *wb0, *wb1, *xbase;
  unsigned wo0, wo1, xo0, xo1, xo2;
  int wstep;
  unsigned xb0, xb1, wb[8];
  unsigned wdst, xdst;
};
struct XF { bf16x8 x[3]; };

template <int S>
__device__ __forceinline__ void read_x(XF& f, const K1State& st) {
  const unsigned xo = ((S >> 1) % 3) * XSLOT;
  const unsigned xb = (S & 1) ? st.xb1 : st.xb0;
#pragma unroll
  for (int i = 0; i < 3; ++i) f.x[i] = lds_read16(xb + xo + i * 2048);
}
template <int S>
__device__ __forceinline__ bf16x8 read_w(const K1State& st, int u) {
  const unsigned wo = W_OFF + (S % 5) * WSLOT;
  return cat(tr_read(st.wb[u] + wo), tr_read(st.wb[u] + wo + 1024));
}

template <int T> __device__ __forceinline__ void issue_w(K1State& st) {
  const unsigned d = W_OFF + (T % 5) * WSLOT + st.wdst;
  glds16(st.wb0 + T * st.wstep, st.wo0, d);
  glds16(st.wb1 + T * st.wstep, st.wo1, d + 8192);
}
template <int J> __device__ __forceinline__ void issue_x(K1State& st) {
  const unsigned d = X_OFF + (J % 3) * XSLOT + st.xdst;
  const char* xb = st.xbase + J * 128;
  glds16(xb, st.xo0, d);
  glds16(xb, st.xo1, d + 1024);
  glds16(xb, st.xo2, d + 2048);
}

template <int S>
__device__ __forceinline__ void step(K1State& st, XF& cur, XF& nxt) {
  if constexpr (S + 5 < NS) issue_w<S + 5>(st);
  if constexpr ((S & 1) && (S + 5) / 2 < NS / 2) issue_x<(S + 5) / 2>(st);
  if constexpr (S + 1 < NS) read_x<S + 1>(nxt, st);
#pragma unroll
  for (int u = 0; u < 8; ++u) {
#pragma unroll
    for (int i = 0; i < 3; ++i)
      st.acc[i][u] = __builtin_amdgcn_mfma_f32_16x16x32_bf16(st.fw[u], cur.x[i], st.acc[i][u], 0, 0, 0);
    if ((u & 3) == 0) {
      const u32x4 v = __builtin_bit_cast(u32x4, st.fw[u]);
#pragma unroll
      for (int r = 0; r < 4; ++r) st.cs[u >> 2] = dot2(v[r], 0x3f803f80u, st.cs[u >> 2]);
      asm volatile("" : "+v"(st.cs[u >> 2]));
    } else {
      const int i = (u & 3) - 1;
      const u32x4 v = __builtin_bit_cast(u32x4, cur.x[i]);
#pragma unroll
      for (int r = 0; r < 4; ++r) {
        if (u < 4) st.sx[i] = dot2(v[r], 0x3f803f80u, st.sx[i]);
        else st.sq[i] = dot2(v[r], v[r], st.sq[i]);
      }
      if (u < 4) asm volatile("" : "+v"(st.sx[i])); else asm volatile("" : "+v"(st.sq[i]));
    }
    if constexpr (S + 1 < NS) st.fw[u] = read_w<S + 1>(st, u);
  }
  if constexpr (S + 2 < NS) {
    wait_vm<outstanding_after(S, S + 2, (S + 2) / 2)>();
    wait_lgkm0();
    bar();
  }
  __builtin_amdgcn_sched_barrier(0);
}
template <int S>
__device__ __forceinline__ void steps(K1State& st, XF& a, XF& b) {
  step<S>(st, a, b);
  step<S + 1>(st, b, a);
  if constexpr (S + 2 < NS) steps<S + 2>(st, a, b);
}

__device__ __forceinline__ float xsum4(float v) {
  v += __shfl_xor(v, 16);
  v += __shfl_xor(v, 32);
  return v;
}
__device__ __forceinline__ float xmax4(float v) {
  v = fmaxf(v, __shfl_xor(v, 16));
  v = fmaxf(v, __shfl_xor(v, 32));
  return v;
}
__device__ __forceinline__ float gelu_mul(float g, float u) {
  const float c1 = -2.0f * 0.7978845608028654f * 1.4426950408889634f;
  const float c2 = c1 * 0.044715f;
  const float z = g * __builtin_fmaf(g * g, c2, c1);
  const float e = __builtin_amdgcn_exp2f(z);
  return (g * u) * __builtin_amdgcn_rcpf(1.0f + e);
}

__global__ __launch_bounds__(512, 2) void k1_kernel(const char* __restrict__ x, const char* __restrict__ wqkv,
                                                     const char* __restrict__ wo, const char* __restrict__ wg,
                                                     const char* __restrict__ wu, const char* __restrict__ wd,
                                                     char* __restrict__ act, char* __restrict__ w2p) {
  extern __shared__ __attribute__((aligned(16))) char smem[];
  const int tid = threadIdx.x, l = tid & 63;
  const int w = __builtin_amdgcn_readfirstlane(tid >> 6);
  const int wm = w & 3, wn = w >> 2;
  const int b = blockIdx.x, xcd = b & 7, idx = b >> 3;
  const bool isA = xcd < 3;
  int rt, ct;
  bool active = true;
  if (isA) {
    const int a = xcd * 32 + idx;
    rt = a >> 3; ct = a & 7;
  } else {
    const int f = (xcd - 3) * 32 + idx;
    if (f < 84) { ct = f / 6; rt = f % 6; }
    else { const int f2 = f - 84; ct = f2 / 5; rt = 6 + f2 % 5; active = f < 154; }
  }
  const int g4 = l >> 4, l15 = l & 15;
  const unsigned sbase = (unsigned)(uintptr_t)(LDS_AS char*)smem;

  if (active) {
    const int t0 = isA ? 176 * rt - 16 : 192 * rt;
    K1State st;
    {
      const int k = 4 * w + g4;
      const int f = g4 | (((w >> 1) & 1) << 2);
      const int c16 = l15 ^ (f << 1);
      if (isA) {
#pragma unroll
        for (int P = 0; P < 2; ++P) {
          int c = 16 * P + c16;
          if (c >= 30) c -= 2;
          const int sec = c / 10, cc = c - sec * 10;
          const int col = (sec == 2 ? 0 : (sec + 1) * 640) + ct * 80 + cc * 8;
          const unsigned o = k * 3840 + col * 2;
          if (P == 0) st.wo0 = o; else st.wo1 = o;
        }
        st.wb0 = wqkv; st.wb1 = wqkv;
      } else {
        int col = ct * 128 + c16 * 8;
        if (col >= F_) col -= 64;
        st.wo0 = st.wo1 = k * 3456 + col * 2;
        st.wb0 = wg; st.wb1 = wu;
      }
      st.wstep = isA ? 32 * 3840 : 32 * 3456;
#pragma unroll
      for (int i = 0; i < 3; ++i) {
        const int row = 8 * (3 * w + i) + (l >> 3);
        const int c = (l & 7) ^ ((row >> 1) & 7);
        int gr = t0 + row;
        gr = gr < 0 ? 0 : (gr > S_ - 1 ? S_ - 1 : gr);
        const unsigned p = gr * 1280 + c * 16;
        if (i == 0) st.xo0 = p; else if (i == 1) st.xo1 = p; else st.xo2 = p;
      }
      st.xbase = x;
      st.wdst = sbase + w * 1024;
      st.xdst = sbase + w * 3072;
    }
    issue_x<0>(st); issue_w<0>(st); issue_w<1>(st); issue_x<1>(st);
    issue_w<2>(st); issue_w<3>(st); issue_x<2>(st); issue_w<4>(st);
    {
      const unsigned swz = l15 >> 1;
      st.xb0 = sbase + X_OFF + (48 * wm + l15) * 128 + 16 * ((unsigned)g4 ^ swz);
      st.xb1 = st.xb0 ^ 64u;
      asm volatile("" : "+v"(st.xb0), "+v"(st.xb1));
      const int q = l15 >> 2, p = l & 3;
      const int f = q | ((g4 & 1) << 2);
      const unsigned wbase = sbase + (8 * g4 + q) * 256 + 16 * (p >> 1) + 8 * (p & 1);
#pragma unroll
      for (int u = 0; u < 8; ++u) {
        const int ua = u ^ wm;
        const int part = isA ? wn : (ua >> 2), pair = isA ? ua : 4 * wn + (ua & 3);
        st.wb[u] = wbase + part * 8192 + 32 * (pair ^ f);
        asm volatile("" : "+v"(st.wb[u]));
      }
    }
#pragma unroll
    for (int i = 0; i < 3; ++i) {
      st.sx[i] = 0.f; st.sq[i] = 0.f;
#pragma unroll
      for (int u = 0; u < 8; ++u) st.acc[i][u] = f32x4{0.f, 0.f, 0.f, 0.f};
    }
    st.cs[0] = st.cs[1] = 0.f;

    XF fa, fb;
    wait_vm<outstanding_after(-1, 0, 0)>();
    bar();
    read_x<0>(fa, st);
#pragma unroll
    for (int u = 0; u < 8; ++u) st.fw[u] = read_w<0>(st, u);
    wait_vm<outstanding_after(-1, 1, 0)>();
    wait_lgkm0();
    bar();
    steps<0>(st, fa, fb);

    float mean[3], rstd[3];
#pragma unroll
    for (int i = 0; i < 3; ++i) {
      const float m = xsum4(st.sx[i]) * (1.0f / D_);
      const float v = fmaxf(xsum4(st.sq[i]) * (1.0f / D_) - m * m, 0.f);
      mean[i] = m;
      rstd[i] = __builtin_amdgcn_rsqf(v + 1e-5f);
    }
    __syncthreads();
    float* csp = (float*)(smem + CS_OFF);
#pragma unroll
    for (int j = 0; j < 2; ++j) {
      const float c = xsum4(st.cs[j]);
      if (l < 16) csp[wn * 128 + (4 * j + wm) * 16 + l] = c;
    }
    __syncthreads();

    if (!isA) {
      const bool colok = ct * 128 + wn * 64 < F_;
#pragma unroll
      for (int grp = 0; grp < 2; ++grp) {
        f32x4 cg[2], cu[2];
#pragma unroll
        for (int a = 0; a < 2; ++a) {
          cg[a] = *(const f32x4*)(csp + wn * 128 + ((2 * grp + a) ^ wm) * 16 + 4 * g4);
          cu[a] = *(const f32x4*)(csp + wn * 128 + (4 + ((2 * grp + a) ^ wm)) * 16 + 4 * g4);
        }
        const int ks = 20 + 4 * ct + 2 * wn + (grp ^ (wm >> 1));
#pragma unroll
        for (int i = 0; i < 3; ++i) {
          float h[8];
#pragma unroll
          for (int a = 0; a < 2; ++a)
#pragma unroll
            for (int r = 0; r < 4; ++r) {
              const float g = rstd[i] * __builtin_fmaf(-mean[i], cg[a][r], st.acc[i][2 * grp + a][r]);
              const float u = rstd[i] * __builtin_fmaf(-mean[i], cu[a][r], st.acc[i][4 + 2 * grp + a][r]);
              h[4 * a + r] = gelu_mul(g, u);
            }
          const int tu = 12 * rt + 3 * wm + i;
          if (colok && tu < 128) {
            const unsigned p0 = pack2(h[0], h[1]), p1 = pack2(h[2], h[3]), p2 = pack2(h[4], h[5]), p3 = pack2(h[6], h[7]);
            const bool sw = wm & 1;
            u32x4 o = {sw ? p2 : p0, sw ? p3 : p1, sw ? p0 : p2, sw ? p1 : p3};
            *(u32x4*)(act + ((long)(tu * KT2 + ks) * 64 + l) * 16) = o;
          }
        }
      }
    } else {
#pragma unroll
      for (int u = 0; u < 8; ++u) {
        const int ua = u ^ wm;
        const f32x4 c = *(const f32x4*)(csp + wn * 128 + ua * 16 + 4 * g4);
        if (ua < 7 || wn == 0) {
#pragma unroll
          for (int i = 0; i < 3; ++i) {
            float v[4];
#pragma unroll
            for (int r = 0; r < 4; ++r) v[r] = rstd[i] * __builtin_fmaf(-mean[i], c[r], st.acc[i][u][r]);
            u32x2 o = {pack2(v[0], v[1]), pack2(v[2], v[3])};
            *(u32x2*)(smem + (16 * (3 * wm + i) + l15) * QROW + (16 * (8 * wn + ua) + 4 * g4) * 2) = o;
          }
        }
      }
      __syncthreads();
      const float sc = 0.22360679774997896f * 1.4426950408889634f;
      const int q4 = l15 >> 2, p4 = l & 3;
      for (int it = w; it < 44; it += 8) {
        const int iu = 1 + (it >> 2), hh = it & 3;
        const int tu = 11 * rt - 1 + iu;
        if (tu >= 128) break;
        const unsigned rq = sbase + (16 * iu + l15) * QROW + 40 * hh + 16 * g4;
        u32x2 q0 = *(LDS_AS u32x2*)(uintptr_t)(rq + 320), q1 = *(LDS_AS u32x2*)(uintptr_t)(rq + 328);
        u32x2 a0 = *(LDS_AS u32x2*)(uintptr_t)(rq - 16 * QROW), a1 = *(LDS_AS u32x2*)(uintptr_t)(rq - 16 * QROW + 8);
        u32x2 b0 = *(LDS_AS u32x2*)(uintptr_t)(rq), b1 = *(LDS_AS u32x2*)(uintptr_t)(rq + 8);
        const unsigned rv = sbase + (16 * iu + 4 * g4 + q4) * QROW + 160 + 40 * hh + 8 * p4;
        const s16x4 v00 = tr_read(rv - 16 * QROW), v01 = tr_read(rv);
        const s16x4 v10 = tr_read(rv - 16 * QROW + 32), v11 = tr_read(rv + 32);
        const bool lo = g4 < 3, hi = g4 < 2;
        const u32x4 qf = {lo ? q0[0] : 0u, lo ? q0[1] : 0u, hi ? q1[0] : 0u, hi ? q1[1] : 0u};
        const u32x4 kp = {lo ? a0[0] : 0u, lo ? a0[1] : 0u, hi ? a1[0] : 0u, hi ? a1[1] : 0u};
        const u32x4 kc = {lo ? b0[0] : 0u, lo ? b0[1] : 0u, hi ? b1[0] : 0u, hi ? b1[1] : 0u};
        const f32x4 z4 = {0.f, 0.f, 0.f, 0.f};
        f32x4 s0 = __builtin_amdgcn_mfma_f32_16x16x32_bf16(__builtin_bit_cast(bf16x8, kp), __builtin_bit_cast(bf16x8, qf), z4, 0, 0, 0);
        f32x4 s1 = __builtin_amdgcn_mfma_f32_16x16x32_bf16(__builtin_bit_cast(bf16x8, kc), __builtin_bit_cast(bf16x8, qf), z4, 0, 0, 0);
        float sv[8];
        float m = -3.0e38f;
#pragma unroll
        for (int r = 0; r < 4; ++r) {
          const int d1 = l15 - 4 * g4 - r, d0 = d1 + 16;
          const bool ok0 = !(d0 & 1) && d0 <= 14 && tu > 0;
          const bool ok1 = !(d1 & 1) && d1 >= 0 && d1 <= 14;
          sv[r] = ok0 ? s0[r] * sc : -3.0e38f;
          sv[4 + r] = ok1 ? s1[r] * sc : -3.0e38f;
          m = fmaxf(m, fmaxf(sv[r], sv[4 + r]));
        }
        m = xmax4(m);
        float sum = 0.f;
#pragma unroll
        for (int r = 0; r < 8; ++r) { sv[r] = __builtin_amdgcn_exp2f(sv[r] - m); sum += sv[r]; }
        const float inv = __builtin_amdgcn_rcpf(xsum4(sum));
        const u32x4 pf = {pack2(sv[0] * inv, sv[1] * inv), pack2(sv[2] * inv, sv[3] * inv),
                          pack2(sv[4] * inv, sv[5] * inv), pack2(sv[6] * inv, sv[7] * inv)};
        const f32x4 o0 = __builtin_amdgcn_mfma_f32_16x16x32_bf16(cat(v00, v01), __builtin_bit_cast(bf16x8, pf), z4, 0, 0, 0);
        const f32x4 o1 = __builtin_amdgcn_mfma_f32_16x16x32_bf16(cat(v10, v11), __builtin_bit_cast(bf16x8, pf), z4, 0, 0, 0);
        {
          const int col = 80 * ct + 20 * hh + 4 * g4;
          u32x2 o = {pack2(o0[0], o0[1]), pack2(o0[2], o0[3])};
          *(u32x2*)(act + ((long)(tu * KT2 + (col >> 5)) * 64 + ((col >> 3) & 3) * 16 + l15) * 16 + (col & 4) * 2) = o;
        }
        if (g4 == 0) {
          const int col = 80 * ct + 20 * hh + 16;
          u32x2 o = {pack2(o1[0], o1[1]), pack2(o1[2], o1[3])};
          *(u32x2*)(act + ((long)(tu * KT2 + (col >> 5)) * 64 + ((col >> 3) & 3) * 16 + l15) * 16 + (col & 4) * 2) = o;
        }
      }
    }
  }

  const int sb = b * 3 + w;
  if (w < 3 && sb < 740) {
    const int ks = sb / 10, nb = sb - ks * 10;
    u32x2 v[8];
#pragma unroll
    for (int j = 0; j < 8; ++j) {
      const char* p = ks < 20 ? wo + (long)(32 * ks + 8 * g4 + j) * 1280
                              : wd + (long)(32 * (ks - 20) + (j < 4 ? 4 * g4 + j : 12 + 4 * g4 + j)) * 1280;
      v[j] = *(const u32x2*)(p + (64 * nb + 4 * l15) * 2);
    }
#pragma unroll
    for (int i = 0; i < 4; ++i) {
      const unsigned sel = (i & 1) ? 0x07060302u : 0x05040100u;
      u32x4 o;
#pragma unroll
      for (int jj = 0; jj < 4; ++jj) o[jj] = __builtin_amdgcn_perm(v[2 * jj + 1][i >> 1], v[2 * jj][i >> 1], sel);
      const int unit = 4 * nb + (l15 >> 2);
      *(u32x4*)(w2p + ((long)(unit * KT2 + ks) * 64 + g4 * 16 + 4 * (l15 & 3) + i) * 16) = o;
    }
  }
}

constexpr int K2D = 6;
__global__ __launch_bounds__(256, 1) void k2_kernel(const char* __restrict__ x, const char* __restrict__ act,
                                                     const char* __restrict__ w2p, char* __restrict__ out) {
  extern __shared__ __attribute__((aligned(16))) char smem[];
  const int tid = threadIdx.x, l = tid & 63;
  const int w = __builtin_amdgcn_readfirstlane(tid >> 6);
  const int b = blockIdx.x, xcd = b & 7, idx = b >> 3;
  const int rt = (xcd >> 1) * 8 + (idx >> 2), ct = (xcd & 1) * 4 + (idx & 3);
  const char* ap = act + ((long)(4 * rt * KT2 + w) * 64 + l) * 16;
  const char* bp = w2p + ((long)(5 * ct * KT2 + w) * 64 + l) * 16;
  const int last = (w < 2) ? 18 : 17;
  bf16x8 fa[K2D][4], fb[K2D][5];
  f32x4 acc[4][5];
#pragma unroll
  for (int i = 0; i < 4; ++i)
#pragma unroll
    for (int u = 0; u < 5; ++u) acc[i][u] = f32x4{0.f, 0.f, 0.f, 0.f};
  const int g4 = l >> 4, l15 = l & 15;
  const long ooff = (long)(16 * (4 * rt + w) + l15) * 1280 + (80 * ct + 4 * g4) * 2;
#pragma unroll
  for (int it = 0; it < K2D; ++it) {
#pragma unroll
    for (int u = 0; u < 5; ++u) fb[it][u] = *(const bf16x8*)(bp + (long)(u * KT2 + 4 * it) * 1024);
#pragma unroll
    for (int i = 0; i < 4; ++i) fa[it][i] = *(const bf16x8*)(ap + (long)(i * KT2 + 4 * it) * 1024);
  }
  u32x2 xr[5];
#pragma unroll
  for (int u = 0; u < 5; ++u) xr[u] = *(const u32x2*)(x + ooff + u * 32);
  __builtin_amdgcn_sched_barrier(0);
#pragma unroll
  for (int it = 0; it < 19; ++it) {
    const int sl = it % K2D;
    if (it < 18 || w < 2) {
#pragma unroll
      for (int u = 0; u < 5; ++u)
#pragma unroll
        for (int i = 0; i < 4; ++i)
          acc[i][u] = __builtin_amdgcn_mfma_f32_16x16x32_bf16(fb[sl][u], fa[sl][i], acc[i][u], 0, 0, 0);
    }
    if (it + K2D < 19) {
      const int nx = (it + K2D) < last ? (it + K2D) : last;
#pragma unroll
      for (int u = 0; u < 5; ++u) fb[sl][u] = *(const bf16x8*)(bp + (long)(u * KT2 + 4 * nx) * 1024);
#pragma unroll
      for (int i = 0; i < 4; ++i) fa[sl][i] = *(const bf16x8*)(ap + (long)(i * KT2 + 4 * nx) * 1024);
    }
    __builtin_amdgcn_sched_barrier(0);
  }
  f32x4* red = (f32x4*)smem;
#pragma unroll
  for (int i = 0; i < 4; ++i)
#pragma unroll
    for (int u = 0; u < 5; ++u) red[((w * 4 + i) * 5 + u) * 64 + l] = acc[i][u];
  __syncthreads();
#pragma unroll
  for (int u = 0; u < 5; ++u) {
    const f32x4 s = (red[((0 * 4 + w) * 5 + u) * 64 + l] + red[((1 * 4 + w) * 5 + u) * 64 + l]) +
                    (red[((2 * 4 + w) * 5 + u) * 64 + l] + red[((3 * 4 + w) * 5 + u) * 64 + l]);
    const float x0 = __builtin_bit_cast(float, xr[u][0] << 16), x1 = __builtin_bit_cast(float, xr[u][0] & 0xffff0000u);
    const float x2 = __builtin_bit_cast(float, xr[u][1] << 16), x3 = __builtin_bit_cast(float, xr[u][1] & 0xffff0000u);
    u32x2 o = {pack2(x0 + s[0], x1 + s[1]), pack2(x2 + s[2], x3 + s[3])};
    *(u32x2*)(out + ooff + u * 32) = o;
  }
}

extern "C" void kernel_launch(void* const* d_in, const int* in_sizes, int n_in, void* d_out, int out_size,
                              void* d_ws, size_t ws_size, hipStream_t stream) {
  const char* x = (const char*)d_in[0];
  const char* wqkv = (const char*)d_in[1];
  const char* wo = (const char*)d_in[2];
  const char* wg = (const char*)d_in[4];
  const char* wu = (const char*)d_in[5];
  const char* wd = (const char*)d_in[6];
  char* act = (char*)d_ws;
  char* w2p = act + (size_t)128 * KT2 * 1024;
  (void)hipFuncSetAttribute((const void*)k1_kernel, hipFuncAttributeMaxDynamicSharedMemorySize, LDS1);
  (void)hipFuncSetAttribute((const void*)k2_kernel, hipFuncAttributeMaxDynamicSharedMemorySize, 81920);
  k1_kernel<<<256, 512, LDS1, stream>>>(x, wqkv, wo, wg, wu, wd, act, w2p);
  k2_kernel<<<256, 256, 81920, stream>>>(x, act, w2p, (char*)d_out);
}
```

```cpp
#include <hip/hip_runtime.h>
#include <hip/hip_bf16.h>
#include <stdint.h>

typedef __attribute__((ext_vector_type(8))) short bf16x8;
typedef __attribute__((ext_vector_type(4))) short s16x4;
typedef __attribute__((ext_vector_type(4))) float f32x4;
typedef __attribute__((ext_vector_type(2))) float f32x2;
typedef __attribute__((ext_vector_type(16))) float f32x16;
typedef __attribute__((ext_vector_type(2))) __bf16 bf2;
typedef __attribute__((ext_vector_type(4))) unsigned u32x4;
typedef __attribute__((ext_vector_type(2))) unsigned u32x2;
#define LDS_AS __attribute__((address_space(3)))
#define STAMP(base, i)
#define STAMPR(base, i)

constexpr int S_ = 2048, D_ = 640, F_ = 1728;
constexpr int KT2 = 74;
constexpr int NS = 20;
constexpr int WSLOT = 16384, XSLOT = 24576;
constexpr int W_OFF = 0, X_OFF = 5 * WSLOT;
constexpr int LDS1 = 5 * WSLOT + 3 * XSLOT;
constexpr int QROW = 488;
constexpr int CS_OFF = 192 * QROW;

__device__ __forceinline__ float dot2(unsigned a, unsigned b, float c) {
  return __builtin_amdgcn_fdot2_f32_bf16(__builtin_bit_cast(bf2, a), __builtin_bit_cast(bf2, b), c, false);
}
__device__ __forceinline__ unsigned pack2(float a, float b) {
  f32x2 p = {a, b};
  return __builtin_bit_cast(unsigned, __builtin_convertvector(p, bf2));
}
__device__ __forceinline__ void glds16(const char* base, unsigned voff, unsigned lds_dst, unsigned lds_off) {
  asm volatile("s_add_u32 m0, %2, %3\n\ts_nop 0\n\tglobal_load_lds_dwordx4 %0, %1" ::"v"(voff), "s"(base), "s"(lds_dst), "s"(lds_off)
               : "memory", "scc");
}
__device__ __forceinline__ s16x4 tr_read(unsigned lds_off) {
  return __builtin_amdgcn_ds_read_tr16_b64_v4i16((LDS_AS s16x4*)(uintptr_t)lds_off);
}
__device__ __forceinline__ bf16x8 lds_read16(unsigned lds_off) { return *(LDS_AS bf16x8*)(uintptr_t)lds_off; }
__device__ __forceinline__ u32x2 lds_read8(unsigned lds_off) { return *(LDS_AS u32x2*)(uintptr_t)lds_off; }
__device__ __forceinline__ bf16x8 cat(s16x4 a, s16x4 b) { return __builtin_shufflevector(a, b, 0, 1, 2, 3, 4, 5, 6, 7); }
template <int N> __device__ __forceinline__ void wait_vm() { asm volatile("s_waitcnt vmcnt(%0)" ::"n"(N) : "memory"); }
__device__ __forceinline__ void wait_lgkm0() { asm volatile("s_waitcnt lgkmcnt(0)" ::: "memory"); }
__device__ __forceinline__ void bar() { __builtin_amdgcn_s_barrier(); }
#define MFMA(a, b, c) __builtin_amdgcn_mfma_f32_16x16x32_bf16(a, b, c, 0, 0, 0)

__device__ __forceinline__ void swap16(float v, float& a, float& b) {
  a = v; b = v;
  asm volatile("s_nop 1\n\tv_permlane16_swap_b32 %0, %1" : "+v"(a), "+v"(b));
}
__device__ __forceinline__ void swap32(float v, float& a, float& b) {
  a = v; b = v;
  asm volatile("s_nop 1\n\tv_permlane32_swap_b32 %0, %1" : "+v"(a), "+v"(b));
}
__device__ __forceinline__ float xsum4(float v) {
  float a, b;
  swap16(v, a, b); v = a + b;
  swap32(v, a, b); return a + b;
}
__device__ __forceinline__ float xmax4(float v) {
  float a, b;
  swap16(v, a, b); v = fmaxf(a, b);
  swap32(v, a, b); return fmaxf(a, b);
}
__device__ __forceinline__ float sum8(float v) {
  v += __builtin_bit_cast(float, __builtin_amdgcn_update_dpp(0, __builtin_bit_cast(int, v), 0xB1, 0xF, 0xF, true));
  v += __builtin_bit_cast(float, __builtin_amdgcn_update_dpp(0, __builtin_bit_cast(int, v), 0x4E, 0xF, 0xF, true));
  v += __builtin_bit_cast(float, __builtin_amdgcn_update_dpp(0, __builtin_bit_cast(int, v), 0x141, 0xF, 0xF, true));
  return v;
}
#define MFMA32(a, b, c) __builtin_amdgcn_mfma_f32_32x32x16_bf16(a, b, c, 0, 0, 0)
#define SB() __builtin_amdgcn_sched_barrier(0)
#define RDW(base, e) cat(tr_read((base) + (e) * 8192), tr_read((base) + (e) * 8192 + 1024))
#define STAT(d) do { const unsigned v_ = sv[(d) >> 2][(d) & 3];                                                    \
    float lo_ = __builtin_bit_cast(float, v_ << 16), hi_ = __builtin_bit_cast(float, v_ & 0xffff0000u);             \
    sx[(d) >> 2] += lo_; sq[(d) >> 2] = __builtin_fmaf(lo_, lo_, sq[(d) >> 2]);                                     \
    asm volatile("" : "+v"(sx[(d) >> 2]), "+v"(sq[(d) >> 2]));                                                      \
    sx[(d) >> 2] += hi_; sq[(d) >> 2] = __builtin_fmaf(hi_, hi_, sq[(d) >> 2]);                                     \
    asm volatile("" : "+v"(sx[(d) >> 2]), "+v"(sq[(d) >> 2])); } while (0)
#define CSUM(e) do { SB(); cs[e] = MFMA(wa[e], sel, cs[e]); } while (0)
#define K1_STEP(EVEN, MORE)                                                                              \
  do {                                                                                                   \
                 \
    const unsigned xh = xb ^ 32u;                                                                        \
    acc[0][0] = MFMA32(wa[0], xa[0], acc[0][0]); wc[0] = RDW(wb1, 0); xc[0] = lds_read16(xh); SB();       \
    acc[1][0] = MFMA32(wa[0], xa[1], acc[1][0]); xc[1] = lds_read16(xh + 4096); xc[2] = lds_read16(xh + 8192); \
    if (EVEN) sv[0] = __builtin_bit_cast(u32x4, lds_read16(sb_)); SB();                                   \
    acc[2][0] = MFMA32(wa[0], xa[2], acc[2][0]); wc[1] = RDW(wb1, 1); CSUM(0); SB();                   \
    acc[0][1] = MFMA32(wa[1], xa[0], acc[0][1]);                                              \
    if (EVEN) { sv[1] = __builtin_bit_cast(u32x4, lds_read16(sb_ + 1024)); sv[2] = __builtin_bit_cast(u32x4, lds_read16(sb_ + 2048)); } \
    else STAT(6);                                                                                        \
    SB();                                                                                                \
    acc[1][1] = MFMA32(wa[1], xa[1], acc[1][1]); if (MORE) glds16(ws0, wo0, wdst, wfill); SB(); \
    acc[2][1] = MFMA32(wa[1], xa[2], acc[2][1]); if (MORE) glds16(ws1, wo1, wdst + 8192, wfill); CSUM(1); \
    wb0 += winc; wb1 += winc; if (EVEN) xb ^= 64u; else { xb = (xb ^ 64u) + xinc; sb_ += xinc; } SB();    \
                      \
    acc[0][0] = MFMA32(wc[0], xc[0], acc[0][0]); wa[0] = RDW(wb0, 0); xa[0] = lds_read16(xb); SB();       \
    acc[1][0] = MFMA32(wc[0], xc[1], acc[1][0]); xa[1] = lds_read16(xb + 4096); xa[2] = lds_read16(xb + 8192); \
    STAT(EVEN ? 0 : 7); SB();                                                                            \
    acc[2][0] = MFMA32(wc[0], xc[2], acc[2][0]); wa[1] = RDW(wb0, 1); STAT(EVEN ? 1 : 8); SB();           \
    acc[0][1] = MFMA32(wc[1], xc[0], acc[0][1]); STAT(EVEN ? 2 : 9);                                      \
    if (EVEN && MORE) glds16(xs, xo[0], xdst, xfill); SB();                                               \
    acc[1][1] = MFMA32(wc[1], xc[1], acc[1][1]); STAT(EVEN ? 3 : 10);                                     \
    if (EVEN && MORE) glds16(xs, xo[1], xdst + 1024, xfill); SB();                                        \
    acc[2][1] = MFMA32(wc[1], xc[2], acc[2][1]); STAT(EVEN ? 4 : 11);                                     \
    if (EVEN && MORE) glds16(xs, xo[2], xdst + 2048, xfill);                                              \
    if (EVEN) STAT(5);                                                                                   \
    SB();                                                                                                \
  } while (0)
#define K1_PAIR(MORE, W0, W1)                                                                            \
  do {                                                                                                   \
    int winc = wfill == 3 * WSLOT ? -4 * WSLOT : WSLOT;                                                  \
    const int xinc = xfill == XSLOT ? -2 * XSLOT : XSLOT;                                                \
    K1_STEP(true, MORE);                                                                                 \
    ws0 += wstep; ws1 += wstep; xs += 128;                                                               \
    wfill = wfill == 4 * WSLOT ? 0 : wfill + WSLOT;                                                      \
    xfill = xfill == 2 * XSLOT ? 0 : xfill + XSLOT;                                                      \
    winc = wfill == 3 * WSLOT ? -4 * WSLOT : WSLOT;                                                      \
    wait_vm<W0>();                                                                                       \
    bar();                                                                                               \
    K1_STEP(false, MORE);                                                                                \
    ws0 += wstep; ws1 += wstep;                                                                          \
    wfill = wfill == 4 * WSLOT ? 0 : wfill + WSLOT;                                                      \
    wait_vm<W1>();                                                                                       \
    bar();                                                                                               \
  } while (0)

__global__ __launch_bounds__(512, 2) void k1_kernel(const char* __restrict__ x, const char* __restrict__ wqkv,
                                                     const char* __restrict__ wo, const char* __restrict__ wg,
                                                     const char* __restrict__ wu, const char* __restrict__ wd,
                                                     char* __restrict__ act, char* __restrict__ w2p) {
  extern __shared__ __attribute__((aligned(16))) char smem[];
  const int tid = threadIdx.x, l = tid & 63;
  const int w = __builtin_amdgcn_readfirstlane(tid >> 6);
  const int wn = w & 3, wm = w >> 2;
  const int b = blockIdx.x, xcd = b & 7, idx = b >> 3;
  const bool isA = xcd < 3;
  const int lin = xcd * 32 + idx, f0 = lin - 96;
  const int fh = f0 >= 84, f1 = fh ? f0 - 84 : f0;
  const int fct = fh ? f1 / 5 : f1 / 6;
  const int ct = isA ? (lin & 7) : fct;
  const int rt = isA ? (lin >> 3) : (fh ? 6 + f1 - fct * 5 : f1 - fct * 6);
  const int ct_ = ct, rt_ = rt;
  STAMP(act, 0); STAMPR(act, 8);
  const int g4 = l >> 4, l15 = l & 15;
  const unsigned sbase = (unsigned)(uintptr_t)(LDS_AS char*)smem;

  if (f0 < 154) {
    const int t0 = isA ? 176 * rt - 16 : 192 * rt;
    unsigned xo[3];
#pragma unroll
    for (int i = 0; i < 3; ++i) {
      const int row = 24 * w + 8 * i + (l >> 3);
      int gr = t0 + row;
      gr = gr < 0 ? 0 : (gr > S_ - 1 ? S_ - 1 : gr);
      xo[i] = gr * 1280 + (((l & 7) ^ ((row >> 1) & 7)) << 4);
    }
    const unsigned xdst = sbase + X_OFF + w * 3072, wdst = sbase + W_OFF + w * 1024;
    const char* xs = x;
#define ISSUE_X(slot_off) do { glds16(xs, xo[0], xdst, slot_off); glds16(xs, xo[1], xdst + 1024, slot_off); \
                               glds16(xs, xo[2], xdst + 2048, slot_off); xs += 128; } while (0)
    ISSUE_X(0);
    const int wstride = isA ? 3840 : 3456;
    unsigned wo0, wo1;
    {
      const int c16 = l15 ^ (g4 << 2);
      const int rowo = (4 * w + g4) * wstride;
      const int a0 = c16 * 16 + (c16 < 10 ? 1280 : 2400) + ct * 160;
      const int a1 = (c16 < 14 ? c16 : c16 - 2) * 16 + (c16 < 4 ? 2656 : -64) + ct * 160;
      const int fc = ct * 256 + c16 * 16;
      const int f = fc >= 2 * F_ ? fc - 128 : fc;
      wo0 = rowo + (isA ? a0 : f);
      wo1 = rowo + (isA ? a1 : f);
    }
    const char *ws0 = isA ? wqkv : wg, *ws1 = isA ? wqkv : wu;
    const int wstep = 32 * wstride;
#define ISSUE_W(slot_off) do { glds16(ws0, wo0, wdst, slot_off); glds16(ws1, wo1, wdst + 8192, slot_off); \
                               ws0 += wstep; ws1 += wstep; } while (0)
    ISSUE_W(0); ISSUE_W(WSLOT);
    const int hf = l >> 5, l31 = l & 31;
    unsigned xb = sbase + X_OFF + (96 * wm + l31) * 128 + 16 * (unsigned)((2 * wm + hf) ^ ((l31 >> 1) & 7));
    unsigned wb0, wb1;
    {
      const int q = l15 >> 2;
      const unsigned wbase = sbase + W_OFF + (8 * hf + q) * 256 + 64 * (wn ^ q) + 32 * (g4 & 1) + 8 * (l & 3);
      wb0 = wbase + wm * 4096;
      wb1 = wbase + (1 - wm) * 4096;
    }
    f32x16 acc[3][2];
    float sx[3], sq[3];
    f32x4 cs[2];
#pragma unroll
    for (int i = 0; i < 3; ++i) {
      sx[i] = 0.f; sq[i] = 0.f;
#pragma unroll
      for (int r = 0; r < 16; ++r) { acc[i][0][r] = 0.f; acc[i][1][r] = 0.f; }
    }
    cs[0] = cs[1] = f32x4{0.f, 0.f, 0.f, 0.f};
    const unsigned s1_ = l15 == (g4 & 1) ? 0x3f803f80u : 0u;
    u32x4 sel_ = {s1_, s1_, s1_, s1_};
    asm volatile("" : "+v"(sel_));
    const bf16x8 sel = __builtin_bit_cast(bf16x8, sel_);
    unsigned sb_ = xdst + l * 16;
    ISSUE_X(XSLOT); ISSUE_W(2 * WSLOT);
    {
      bf16x8 xa[3], xc[3], wa[2], wc[2];
      u32x4 sv[3];
      wait_vm<7>();
      bar();
      STAMP(act, 1);
      ISSUE_W(3 * WSLOT);
#pragma unroll
      for (int i = 0; i < 3; ++i) xa[i] = lds_read16(xb + i * 4096);
      wa[0] = RDW(wb0, 0); wa[1] = RDW(wb0, 1);
      wait_vm<7>();
      bar();
      int wfill = 4 * WSLOT, xfill = 2 * XSLOT;
#pragma nounroll
      for (int s = 0; s < 16; s += 2) K1_PAIR(true, 7, 7);
#pragma nounroll
      for (int s = 16; s < NS; s += 2) K1_PAIR(false, 2, 0);
      wait_lgkm0();
      bar();
      STAMP(act, 2);
    }
    {
      int l = tid & 63, wn = w & 3, wm = w >> 2, ct = ct_, rt = rt_;
      asm volatile("" : "+v"(l), "+s"(wm), "+s"(wn), "+s"(ct), "+s"(rt));
      const int g4 = l >> 4, l15 = l & 15, hf = l >> 5, l31 = l & 31;
      float* csp = (float*)(smem + CS_OFF);
#pragma unroll
      for (int i = 0; i < 3; ++i) {
        const float m = sum8(sx[i]) * (1.0f / D_);
        const float v = fmaxf(sum8(sq[i]) * (1.0f / D_) - m * m, 0.f) + 1e-5f;
        if ((l & 7) == 0) *(f32x2*)(csp + 1024 + 2 * (24 * w + 8 * i + (l >> 3))) = f32x2{m, v};
      }
      if (l15 < 2) {
        *(f32x4*)(csp + wm * 256 + 32 * wn + 16 * l15 + 4 * g4) = cs[0];
        *(f32x4*)(csp + wm * 256 + 128 + 32 * wn + 16 * l15 + 4 * g4) = cs[1];
      }
      __syncthreads();
      float mean[3], rstd[3], vare[3];
#pragma unroll
      for (int i = 0; i < 3; ++i) {
        const f32x2 mv = *(const f32x2*)(csp + 1024 + 2 * (96 * wm + 32 * i + l31));
        mean[i] = mv[0]; vare[i] = mv[1];
        rstd[i] = __builtin_amdgcn_rsqf(mv[1]);
      }
      const float* cq = csp + 32 * wn + 4 * hf;
#define COLSUM(e, rq) (*(const f32x4*)(cq + 128 * (e) + 8 * (rq)) + *(const f32x4*)(cq + 256 + 128 * (e) + 8 * (rq)))
#define SUB4(v, rq) f32x4{(v)[4 * (rq)], (v)[4 * (rq) + 1], (v)[4 * (rq) + 2], (v)[4 * (rq) + 3]}
      if (!isA) {
        const bool colok = ct * 128 + wn * 32 < F_;
        const float c1 = -2.0f * 0.7978845608028654f * 1.4426950408889634f, c3 = c1 * 0.044715f;
        const int ks = 20 + 4 * ct + wn;
#pragma unroll
        for (int half = 0; half < 2; ++half) {
          f32x4 cg[2], cu[2];
#pragma unroll
          for (int a = 0; a < 2; ++a) { cg[a] = COLSUM(0, 2 * half + a); cu[a] = COLSUM(1, 2 * half + a); }
#pragma unroll
          for (int i = 0; i < 3; ++i) {
            const float k1 = c1 * rstd[i], k3 = c3 * rstd[i] * rstd[i] * rstd[i];
            unsigned pk[4];
#pragma unroll
            for (int a = 0; a < 2; ++a) {
              const f32x4 g = SUB4(acc[i][0], 2 * half + a) - mean[i] * cg[a];
              const f32x4 u = SUB4(acc[i][1], 2 * half + a) - mean[i] * cu[a];
              const f32x4 z = g * (g * g * k3 + k1);
              f32x4 e;
#pragma unroll
              for (int r = 0; r < 4; ++r) e[r] = __builtin_amdgcn_exp2f(z[r]);
              e = e * vare[i] + vare[i];
#pragma unroll
              for (int r = 0; r < 4; ++r) e[r] = __builtin_amdgcn_rcpf(e[r]);
              const f32x4 h = (g * u) * e;
              pk[2 * a] = pack2(h[0], h[1]); pk[2 * a + 1] = pack2(h[2], h[3]);
            }
            const int tu = 12 * rt + 6 * wm + 2 * i;
            if (colok && tu < 128)
              *(u32x4*)(act + (long)(tu * KT2 + ks) * 1024 + (l31 >> 4) * (KT2 * 1024) + (2 * half + hf) * 256 + l15 * 16) =
                  u32x4{pk[0], pk[1], pk[2], pk[3]};
          }
        }
      } else {
#pragma unroll
        for (int e = 0; e < 2; ++e)
#pragma unroll
          for (int rq = 0; rq < 4; ++rq) {
            const f32x4 c = COLSUM(e, rq);
            if (e == 0 || wn < 3 || rq < 2) {
#pragma unroll
              for (int i = 0; i < 3; ++i) {
                const f32x4 v = (SUB4(acc[i][e], rq) - mean[i] * c) * rstd[i];
                *(u32x2*)(smem + (96 * wm + 32 * i + l31) * QROW + (128 * e + 32 * wn + 8 * rq + 4 * hf) * 2) =
                    u32x2{pack2(v[0], v[1]), pack2(v[2], v[3])};
              }
            }
          }
        __syncthreads();
        const float sc = 0.22360679774997896f * 1.4426950408889634f;
        const bool lo = g4 < 3, hi = g4 < 2;
        const int dq = l15 - 4 * g4;
        const int nit = rt == 11 ? 14 : 22;
#pragma nounroll
        for (int it = w; it < nit; it += 8) {
          const int iu = 1 + (it >> 1);
          const int tu = 11 * rt - 1 + iu;
          const f32x4 z4 = {0.f, 0.f, 0.f, 0.f};
          u32x2 oa[2], ob[2];
#pragma unroll
          for (int e = 0; e < 2; ++e) {
            const int hh = 2 * (it & 1) + e;
            const unsigned rq = sbase + (16 * iu + l15) * QROW + 40 * hh + 16 * g4;
            const u32x2 q0 = lds_read8(rq + 320), q1 = lds_read8(rq + 328);
            const u32x2 a0 = lds_read8(rq - 16 * QROW), a1 = lds_read8(rq - 16 * QROW + 8);
            const u32x2 b0 = lds_read8(rq), b1 = lds_read8(rq + 8);
            const unsigned rv = sbase + (16 * iu + (l >> 2)) * QROW + 160 + 40 * hh + 8 * (l & 3);
            const s16x4 v00 = tr_read(rv - 16 * QROW), v01 = tr_read(rv);
            const s16x4 v10 = tr_read(rv - 16 * QROW + 32), v11 = tr_read(rv + 32);
            const u32x4 qf = {lo ? q0[0] : 0u, lo ? q0[1] : 0u, hi ? q1[0] : 0u, hi ? q1[1] : 0u};
            const u32x4 kp = {lo ? a0[0] : 0u, lo ? a0[1] : 0u, hi ? a1[0] : 0u, hi ? a1[1] : 0u};
            const u32x4 kc = {lo ? b0[0] : 0u, lo ? b0[1] : 0u, hi ? b1[0] : 0u, hi ? b1[1] : 0u};
            const f32x4 s0 = MFMA(__builtin_bit_cast(bf16x8, kp), __builtin_bit_cast(bf16x8, qf), z4);
            const f32x4 s1 = MFMA(__builtin_bit_cast(bf16x8, kc), __builtin_bit_cast(bf16x8, qf), z4);
            float sv[4];
            float m = -3.0e38f;
#pragma unroll
            for (int r = 0; r < 4; ++r) {
              const int d1 = dq - r;
              const bool cur = d1 >= 0;
              const bool ok = !(d1 & 1) && (cur ? d1 <= 14 : (d1 <= -2 && tu > 0));
              sv[r] = ok ? (cur ? s1[r] : s0[r]) * sc : -3.0e38f;
              m = fmaxf(m, sv[r]);
            }
            m = xmax4(m);
            float sum = 0.f;
#pragma unroll
            for (int r = 0; r < 4; ++r) { sv[r] = __builtin_amdgcn_exp2f(sv[r] - m); sum += sv[r]; }
            const float inv = __builtin_amdgcn_rcpf(xsum4(sum));
            float pp[4], pc[4];
#pragma unroll
            for (int r = 0; r < 4; ++r) {
              const float pv = sv[r] * inv;
              pc[r] = dq - r >= 0 ? pv : 0.f;
              pp[r] = dq - r >= 0 ? 0.f : pv;
            }
            const u32x4 pf = {pack2(pp[0], pp[1]), pack2(pp[2], pp[3]), pack2(pc[0], pc[1]), pack2(pc[2], pc[3])};
            const f32x4 o0 = MFMA(cat(v00, v01), __builtin_bit_cast(bf16x8, pf), z4);
            const f32x4 o1 = MFMA(cat(v10, v11), __builtin_bit_cast(bf16x8, pf), z4);
            oa[e] = u32x2{pack2(o0[0], o0[1]), pack2(o0[2], o0[3])};
            ob[e] = u32x2{pack2(o1[0], o1[1]), pack2(o1[2], o1[3])};
            asm volatile("" : "+v"(oa[e]), "+v"(ob[e]));
          }
          {
            char* ab = act + (long)tu * (KT2 * 1024) + l15 * 16;
#pragma unroll
            for (int e = 0; e < 2; ++e) {
              const int c0 = 80 * ct + 40 * (it & 1) + 20 * e;
              const int col = c0 + 4 * g4, col2 = c0 + 16;
              *(u32x2*)(ab + (col >> 3) * 256 + (col & 4) * 2) = oa[e];
              if (g4 == 0) *(u32x2*)(ab + (col2 >> 3) * 256 + (col2 & 4) * 2) = ob[e];
            }
          }
        }
      }
    }
  }

  STAMP(act, 3);
  const int sb = b * 3 + w;
  if (w < 3 && sb < 740) {
    const int ks = sb / 10, nb = sb - ks * 10;
    const char* p = (ks < 20 ? wo + (long)(32 * ks) * 1280 : wd + (long)(32 * (ks - 20)) * 1280) + 128 * nb;
    const unsigned ro = (ks < 20 ? 8 * g4 : 16 * (g4 >> 1) + 4 * (g4 & 1)) * 1280 + 8 * l15;
    const unsigned r4 = ro + (ks < 20 ? 4 : 8) * 1280;
    u32x2 v[8];
#pragma unroll
    for (int j = 0; j < 8; ++j) v[j] = *(const u32x2*)(p + ((j < 4 ? ro : r4) + (j & 3) * 1280));
    char* dst = w2p + (long)((4 * nb + (l15 >> 2)) * KT2 + ks) * 1024 + g4 * 256 + (l15 & 3) * 64;
#pragma unroll
    for (int i = 0; i < 4; ++i) {
      const unsigned sel = (i & 1) ? 0x07060302u : 0x05040100u;
      u32x4 o;
#pragma unroll
      for (int jj = 0; jj < 4; ++jj) o[jj] = __builtin_amdgcn_perm(v[2 * jj + 1][i >> 1], v[2 * jj][i >> 1], sel);
      *(u32x4*)(dst + i * 16) = o;
    }
  }
  STAMP(act, 4); STAMPR(act, 9);
}

constexpr int K2D = 3;
__global__ __launch_bounds__(256, 1) void k2_kernel(const char* __restrict__ x, const char* __restrict__ act,
                                                     const char* __restrict__ w2p, char* __restrict__ out) {
  extern __shared__ __attribute__((aligned(16))) char smem[];
  const int tid = threadIdx.x, l = tid & 63;
  const int w = __builtin_amdgcn_readfirstlane(tid >> 6);
  const int b = blockIdx.x, xcd = b & 7, idx = b >> 3;
  const int rt = (xcd >> 1) * 8 + (idx >> 2), ct = (xcd & 1) * 4 + (idx & 3);
  const char* ap = act + (long)(4 * rt * KT2 + w) * 1024;
  const char* bp = w2p + (long)(5 * ct * KT2 + w) * 1024;
  const unsigned lo = l * 16;
  STAMP(act + (1 << 20), 0); STAMPR(act + (1 << 20), 8);
  bf16x8 fa[K2D][4], fb[K2D][5];
  f32x4 acc[4][5];
#define K2_LOAD(d)                                                                                    \
  do {                                                                                                \
    _Pragma("unroll") for (int u = 0; u < 5; ++u) fb[d][u] = *(const bf16x8*)(bp + (lo + u * (KT2 * 1024))); \
    _Pragma("unroll") for (int i = 0; i < 4; ++i) fa[d][i] = *(const bf16x8*)(ap + (lo + i * (KT2 * 1024))); \
    ap += 4096; bp += 4096;                                                                           \
  } while (0)
#define K2_MMA(d)                                                                                     \
  do {                                                                                                \
    _Pragma("unroll") for (int u = 0; u < 5; ++u)                                                     \
    _Pragma("unroll") for (int i = 0; i < 4; ++i)                                                     \
      acc[i][u] = __builtin_amdgcn_mfma_f32_16x16x32_bf16(fb[d][u], fa[d][i], acc[i][u], 0, 0, 0);    \
  } while (0)
#pragma unroll
  for (int d = 0; d < K2D; ++d) K2_LOAD(d);
#pragma unroll
  for (int i = 0; i < 4; ++i)
#pragma unroll
    for (int u = 0; u < 5; ++u) acc[i][u] = f32x4{0.f, 0.f, 0.f, 0.f};
  const int g4 = l >> 4, l15 = l & 15;
  const unsigned ooff = (16 * (4 * rt + w) + l15) * 1280 + (80 * ct + 4 * g4) * 2;
  __builtin_amdgcn_sched_barrier(0);
  STAMP(act + (1 << 20), 1);
#pragma nounroll
  for (int r = 0; r < 5; ++r) {
#pragma unroll
    for (int d = 0; d < K2D; ++d) {
      K2_MMA(d);
      K2_LOAD(d);
      __builtin_amdgcn_sched_barrier(0);
    }
  }
  STAMP(act + (1 << 20), 2);
  K2_MMA(0);
  if (w < 2) K2_LOAD(0);
  u32x2 xr[5];
#pragma unroll
  for (int u = 0; u < 5; ++u) xr[u] = *(const u32x2*)(x + (ooff + u * 32));
  __builtin_amdgcn_sched_barrier(0);
  K2_MMA(1);
  K2_MMA(2);
  if (w < 2) K2_MMA(0);
  STAMP(act + (1 << 20), 3);
  f32x4* red = (f32x4*)smem;
#pragma unroll
  for (int i = 0; i < 4; ++i)
#pragma unroll
    for (int u = 0; u < 5; ++u) red[((w * 4 + i) * 5 + u) * 64 + l] = acc[i][u];
  __syncthreads();
#pragma unroll
  for (int u = 0; u < 5; ++u) {
    const f32x4 s = (red[((0 * 4 + w) * 5 + u) * 64 + l] + red[((1 * 4 + w) * 5 + u) * 64 + l]) +
                    (red[((2 * 4 + w) * 5 + u) * 64 + l] + red[((3 * 4 + w) * 5 + u) * 64 + l]);
    const float x0 = __builtin_bit_cast(float, xr[u][0] << 16), x1 = __builtin_bit_cast(float, xr[u][0] & 0xffff0000u);
    const float x2 = __builtin_bit_cast(float, xr[u][1] << 16), x3 = __builtin_bit_cast(float, xr[u][1] & 0xffff0000u);
    u32x2 o = {pack2(x0 + s[0], x1 + s[1]), pack2(x2 + s[2], x3 + s[3])};
    *(u32x2*)(out + (ooff + u * 32)) = o;
  }
  STAMP(act + (1 << 20), 4); STAMPR(act + (1 << 20), 9);
}

extern "C" void kernel_launch(void* const* d_in, const int* in_sizes, int n_in, void* d_out, int out_size,
                              void* d_ws, size_t ws_size, hipStream_t stream) {
  const char* x = (const char*)d_in[0];
  const char* wqkv = (const char*)d_in[1];
  const char* wo = (const char*)d_in[2];
  const char* wg = (const char*)d_in[4];
  const char* wu = (const char*)d_in[5];
  const char* wd = (const char*)d_in[6];
  char* act = (char*)d_ws;
  char* w2p = act + (size_t)128 * KT2 * 1024;
  (void)hipFuncSetAttribute((const void*)k1_kernel, hipFuncAttributeMaxDynamicSharedMemorySize, LDS1);
  (void)hipFuncSetAttribute((const void*)k2_kernel, hipFuncAttributeMaxDynamicSharedMemorySize, 81920);
  k1_kernel<<<256, 512, LDS1, stream>>>(x, wqkv, wo, wg, wu, wd, act, w2p);
  k2_kernel<<<256, 256, 81920, stream>>>(x, act, w2p, (char*)d_out);
}
```

```cpp
#include <hip/hip_runtime.h>
#include <hip/hip_bf16.h>
#include <stdint.h>

typedef __attribute__((ext_vector_type(8))) short bf16x8;
typedef __attribute__((ext_vector_type(4))) short s16x4;
typedef __attribute__((ext_vector_type(4))) float f32x4;
typedef __attribute__((ext_vector_type(2))) float f32x2;
typedef __attribute__((ext_vector_type(16))) float f32x16;
typedef __attribute__((ext_vector_type(2))) __bf16 bf2;
typedef __attribute__((ext_vector_type(4))) unsigned u32x4;
typedef __attribute__((ext_vector_type(2))) unsigned u32x2;
#define LDS_AS __attribute__((address_space(3)))
#define STAMP(base, i)
#define STAMPR(base, i)

constexpr int S_ = 2048, D_ = 640, F_ = 1728;
constexpr int KT2 = 74;
constexpr int NS = 20;
constexpr int WSLOT = 16384, XSLOT = 24576;
constexpr int W_OFF = 0, X_OFF = 5 * WSLOT;
constexpr int LDS1 = 5 * WSLOT + 3 * XSLOT;
constexpr int QROW = 488;
constexpr int CS_OFF = 192 * QROW;

__device__ __forceinline__ float dot2(unsigned a, unsigned b, float c) {
  return __builtin_amdgcn_fdot2_f32_bf16(__builtin_bit_cast(bf2, a), __builtin_bit_cast(bf2, b), c, false);
}
__device__ __forceinline__ unsigned pack2(float a, float b) {
  f32x2 p = {a, b};
  return __builtin_bit_cast(unsigned, __builtin_convertvector(p, bf2));
}
__device__ __forceinline__ void glds16(const char* base, unsigned voff, unsigned lds_dst, unsigned lds_off) {
  asm volatile("s_add_u32 m0, %2, %3\n\ts_nop 0\n\tglobal_load_lds_dwordx4 %0, %1" ::"v"(voff), "s"(base), "s"(lds_dst), "s"(lds_off)
               : "memory", "scc");
}
__device__ __forceinline__ s16x4 tr_read(unsigned lds_off) {
  return __builtin_amdgcn_ds_read_tr16_b64_v4i16((LDS_AS s16x4*)(uintptr_t)lds_off);
}
__device__ __forceinline__ bf16x8 lds_read16(unsigned lds_off) { return *(LDS_AS bf16x8*)(uintptr_t)lds_off; }
__device__ __forceinline__ u32x2 lds_read8(unsigned lds_off) { return *(LDS_AS u32x2*)(uintptr_t)lds_off; }
__device__ __forceinline__ bf16x8 cat(s16x4 a, s16x4 b) { return __builtin_shufflevector(a, b, 0, 1, 2, 3, 4, 5, 6, 7); }
template <int N> __device__ __forceinline__ void wait_vm() { asm volatile("s_waitcnt vmcnt(%0)" ::"n"(N) : "memory"); }
__device__ __forceinline__ void wait_lgkm0() { asm volatile("s_waitcnt lgkmcnt(0)" ::: "memory"); }
__device__ __forceinline__ void bar() { __builtin_amdgcn_s_barrier(); }
#define MFMA(a, b, c) __builtin_amdgcn_mfma_f32_16x16x32_bf16(a, b, c, 0, 0, 0)

__device__ __forceinline__ void swap16(float v, float& a, float& b) {
  a = v; b = v;
  asm volatile("s_nop 1\n\tv_permlane16_swap_b32 %0, %1" : "+v"(a), "+v"(b));
}
__device__ __forceinline__ void swap32(float v, float& a, float& b) {
  a = v; b = v;
  asm volatile("s_nop 1\n\tv_permlane32_swap_b32 %0, %1" : "+v"(a), "+v"(b));
}
__device__ __forceinline__ float xsum4(float v) {
  float a, b;
  swap16(v, a, b); v = a + b;
  swap32(v, a, b); return a + b;
}
__device__ __forceinline__ float xmax4(float v) {
  float a, b;
  swap16(v, a, b); v = fmaxf(a, b);
  swap32(v, a, b); return fmaxf(a, b);
}
__device__ __forceinline__ float sum8(float v) {
  v += __builtin_bit_cast(float, __builtin_amdgcn_update_dpp(0, __builtin_bit_cast(int, v), 0xB1, 0xF, 0xF, true));
  v += __builtin_bit_cast(float, __builtin_amdgcn_update_dpp(0, __builtin_bit_cast(int, v), 0x4E, 0xF, 0xF, true));
  v += __builtin_bit_cast(float, __builtin_amdgcn_update_dpp(0, __builtin_bit_cast(int, v), 0x141, 0xF, 0xF, true));
  return v;
}
#define MFMA32(a, b, c) __builtin_amdgcn_mfma_f32_32x32x16_bf16(a, b, c, 0, 0, 0)
#define SB() __builtin_amdgcn_sched_barrier(0)
#define RDW(base, e) cat(tr_read((base) + (e) * 8192), tr_read((base) + (e) * 8192 + 1024))
#define STAT(d) do { const unsigned v_ = sv[(d) >> 2][(d) & 3];                                                    \
    float lo_ = __builtin_bit_cast(float, v_ << 16), hi_ = __builtin_bit_cast(float, v_ & 0xffff0000u);             \
    sx[(d) >> 2] += lo_; sq[(d) >> 2] = __builtin_fmaf(lo_, lo_, sq[(d) >> 2]);                                     \
    asm volatile("" : "+v"(sx[(d) >> 2]), "+v"(sq[(d) >> 2]));                                                      \
    sx[(d) >> 2] += hi_; sq[(d) >> 2] = __builtin_fmaf(hi_, hi_, sq[(d) >> 2]);                                     \
    asm volatile("" : "+v"(sx[(d) >> 2]), "+v"(sq[(d) >> 2])); } while (0)
#define CSUM(e) do { SB(); cs[e] = MFMA(wa[e], sel, cs[e]); } while (0)
#define K1_STEP(EVEN, MORE)                                                                              \
  do {                                                                                                   \
                 \
    const unsigned xh = xb ^ 32u;                                                                        \
    acc[0][0] = MFMA32(wa[0], xa[0], acc[0][0]); wc[0] = RDW(wb1, 0); xc[0] = lds_read16(xh); SB();       \
    acc[1][0] = MFMA32(wa[0], xa[1], acc[1][0]); xc[1] = lds_read16(xh + 4096); xc[2] = lds_read16(xh + 8192); \
    if (EVEN) sv[0] = __builtin_bit_cast(u32x4, lds_read16(sb_)); SB();                                   \
    acc[2][0] = MFMA32(wa[0], xa[2], acc[2][0]); wc[1] = RDW(wb1, 1); CSUM(0); SB();                   \
    acc[0][1] = MFMA32(wa[1], xa[0], acc[0][1]);                                              \
    if (EVEN) { sv[1] = __builtin_bit_cast(u32x4, lds_read16(sb_ + 1024)); sv[2] = __builtin_bit_cast(u32x4, lds_read16(sb_ + 2048)); } \
    else STAT(6);                                                                                        \
    SB();                                                                                                \
    acc[1][1] = MFMA32(wa[1], xa[1], acc[1][1]); if (MORE) glds16(ws0, wo0, wdst, wfill); SB(); \
    acc[2][1] = MFMA32(wa[1], xa[2], acc[2][1]); if (MORE) glds16(ws1, wo1, wdst + 8192, wfill); CSUM(1); \
    wb0 += winc; wb1 += winc; if (EVEN) xb ^= 64u; else { xb = (xb ^ 64u) + xinc; sb_ += xinc; } SB();    \
                      \
    acc[0][0] = MFMA32(wc[0], xc[0], acc[0][0]); wa[0] = RDW(wb0, 0); xa[0] = lds_read16(xb); SB();       \
    acc[1][0] = MFMA32(wc[0], xc[1], acc[1][0]); xa[1] = lds_read16(xb + 4096); xa[2] = lds_read16(xb + 8192); \
    STAT(EVEN ? 0 : 7); SB();                                                                            \
    acc[2][0] = MFMA32(wc[0], xc[2], acc[2][0]); wa[1] = RDW(wb0, 1); STAT(EVEN ? 1 : 8); SB();           \
    acc[0][1] = MFMA32(wc[1], xc[0], acc[0][1]); STAT(EVEN ? 2 : 9);                                      \
    if (EVEN && MORE) glds16(xs, xo[0], xdst, xfill); SB();                                               \
    acc[1][1] = MFMA32(wc[1], xc[1], acc[1][1]); STAT(EVEN ? 3 : 10);                                     \
    if (EVEN && MORE) glds16(xs, xo[1], xdst + 1024, xfill); SB();                                        \
    acc[2][1] = MFMA32(wc[1], xc[2], acc[2][1]); STAT(EVEN ? 4 : 11);                                     \
    if (EVEN && MORE) glds16(xs, xo[2], xdst + 2048, xfill);                                              \
    if (EVEN) STAT(5);                                                                                   \
    SB();                                                                                                \
  } while (0)
#define K1_PAIR(MORE, W0, W1)                                                                            \
  do {                                                                                                   \
    int winc = wfill == 3 * WSLOT ? -4 * WSLOT : WSLOT;                                                  \
    const int xinc = xfill == XSLOT ? -2 * XSLOT : XSLOT;                                                \
    K1_STEP(true, MORE);                                                                                 \
    ws0 += wstep; ws1 += wstep; xs += 128;                                                               \
    wfill = wfill == 4 * WSLOT ? 0 : wfill + WSLOT;                                                      \
    xfill = xfill == 2 * XSLOT ? 0 : xfill + XSLOT;                                                      \
    winc = wfill == 3 * WSLOT ? -4 * WSLOT : WSLOT;                                                      \
    wait_vm<W0>();                                                                                       \
    bar();                                                                                               \
    K1_STEP(false, MORE);                                                                                \
    ws0 += wstep; ws1 += wstep;                                                                          \
    wfill = wfill == 4 * WSLOT ? 0 : wfill + WSLOT;                                                      \
    wait_vm<W1>();                                                                                       \
    bar();                                                                                               \
  } while (0)

__global__ __launch_bounds__(512, 2) void k1_kernel(const char* __restrict__ x, const char* __restrict__ wqkv,
                                                     const char* __restrict__ wo, const char* __restrict__ wg,
                                                     const char* __restrict__ wu, const char* __restrict__ wd,
                                                     char* __restrict__ act, char* __restrict__ w2p) {
  extern __shared__ __attribute__((aligned(16))) char smem[];
  const int tid = threadIdx.x, l = tid & 63;
  const int w = __builtin_amdgcn_readfirstlane(tid >> 6);
  const int wn = w & 3, wm = w >> 2;
  const int b = blockIdx.x, xcd = b & 7, idx = b >> 3;
  const bool isA = xcd < 3;
  const int lin = xcd * 32 + idx, f0 = lin - 96;
  const int fh = f0 >= 84, f1 = fh ? f0 - 84 : f0;
  const int fct = fh ? f1 / 5 : f1 / 6;
  const int ct = isA ? (lin & 7) : fct;
  const int rt = isA ? (lin >> 3) : (fh ? 6 + f1 - fct * 5 : f1 - fct * 6);
  const int ct_ = ct, rt_ = rt;
  STAMP(act, 0); STAMPR(act, 8);
  const int g4 = l >> 4, l15 = l & 15;
  const unsigned sbase = (unsigned)(uintptr_t)(LDS_AS char*)smem;

  const int psb = f0 * 5 + w;
  const bool dopack = !isA && w < 5 && psb < 740;
  const int pks = psb / 10, pnb = psb - pks * 10;
  u32x2 pv[8];
#define PACK_LOAD()                                                                                                     \
  do {                                                                                                                  \
    const char* p_ = (pks < 20 ? wo + (long)(32 * pks) * 1280 : wd + (long)(32 * (pks - 20)) * 1280) + 128 * pnb;       \
    const unsigned ro = (pks < 20 ? 8 * g4 : 16 * (g4 >> 1) + 4 * (g4 & 1)) * 1280 + 8 * l15;                           \
    const unsigned r4 = ro + (pks < 20 ? 4 : 8) * 1280;                                                                 \
    _Pragma("unroll") for (int j = 0; j < 8; ++j) pv[j] = *(const u32x2*)(p_ + ((j < 4 ? ro : r4) + (j & 3) * 1280));   \
  } while (0)

  if (f0 < 154) {
    const int t0 = isA ? 176 * rt - 16 : 192 * rt;
    unsigned xo[3];
#pragma unroll
    for (int i = 0; i < 3; ++i) {
      const int row = 24 * w + 8 * i + (l >> 3);
      int gr = t0 + row;
      gr = gr < 0 ? 0 : (gr > S_ - 1 ? S_ - 1 : gr);
      xo[i] = gr * 1280 + (((l & 7) ^ ((row >> 1) & 7)) << 4);
    }
    const unsigned xdst = sbase + X_OFF + w * 3072, wdst = sbase + W_OFF + w * 1024;
    const char* xs = x;
#define ISSUE_X(slot_off) do { glds16(xs, xo[0], xdst, slot_off); glds16(xs, xo[1], xdst + 1024, slot_off); \
                               glds16(xs, xo[2], xdst + 2048, slot_off); xs += 128; } while (0)
    ISSUE_X(0);
    const int wstride = isA ? 3840 : 3456;
    unsigned wo0, wo1;
    {
      const int c16 = l15 ^ (g4 << 2);
      const int rowo = (4 * w + g4) * wstride;
      const int a0 = c16 * 16 + (c16 < 10 ? 1280 : 2400) + ct * 160;
      const int a1 = (c16 < 14 ? c16 : c16 - 2) * 16 + (c16 < 4 ? 2656 : -64) + ct * 160;
      const int fc = ct * 256 + c16 * 16;
      const int f = fc >= 2 * F_ ? fc - 128 : fc;
      wo0 = rowo + (isA ? a0 : f);
      wo1 = rowo + (isA ? a1 : f);
    }
    const char *ws0 = isA ? wqkv : wg, *ws1 = isA ? wqkv : wu;
    const int wstep = 32 * wstride;
#define ISSUE_W(slot_off) do { glds16(ws0, wo0, wdst, slot_off); glds16(ws1, wo1, wdst + 8192, slot_off); \
                               ws0 += wstep; ws1 += wstep; } while (0)
    ISSUE_W(0); ISSUE_W(WSLOT);
    const int hf = l >> 5, l31 = l & 31;
    unsigned xb = sbase + X_OFF + (96 * wm + l31) * 128 + 16 * (unsigned)((2 * wm + hf) ^ ((l31 >> 1) & 7));
    unsigned wb0, wb1;
    {
      const int q = l15 >> 2;
      const unsigned wbase = sbase + W_OFF + (8 * hf + q) * 256 + 64 * (wn ^ q) + 32 * (g4 & 1) + 8 * (l & 3);
      wb0 = wbase + wm * 4096;
      wb1 = wbase + (1 - wm) * 4096;
    }
    f32x16 acc[3][2];
    float sx[3], sq[3];
    f32x4 cs[2];
#pragma unroll
    for (int i = 0; i < 3; ++i) {
      sx[i] = 0.f; sq[i] = 0.f;
#pragma unroll
      for (int r = 0; r < 16; ++r) { acc[i][0][r] = 0.f; acc[i][1][r] = 0.f; }
    }
    cs[0] = cs[1] = f32x4{0.f, 0.f, 0.f, 0.f};
    const unsigned s1_ = l15 == (g4 & 1) ? 0x3f803f80u : 0u;
    u32x4 sel_ = {s1_, s1_, s1_, s1_};
    asm volatile("" : "+v"(sel_));
    const bf16x8 sel = __builtin_bit_cast(bf16x8, sel_);
    unsigned sb_ = xdst + l * 16;
    ISSUE_X(XSLOT); ISSUE_W(2 * WSLOT);
    {
      bf16x8 xa[3], xc[3], wa[2], wc[2];
      u32x4 sv[3];
      wait_vm<7>();
      bar();
      STAMP(act, 1);
      ISSUE_W(3 * WSLOT);
#pragma unroll
      for (int i = 0; i < 3; ++i) xa[i] = lds_read16(xb + i * 4096);
      wa[0] = RDW(wb0, 0); wa[1] = RDW(wb0, 1);
      wait_vm<7>();
      bar();
      int wfill = 4 * WSLOT, xfill = 2 * XSLOT;
#pragma nounroll
      for (int s = 0; s < 16; s += 2) K1_PAIR(true, 7, 7);
#pragma nounroll
      for (int s = 16; s < NS; s += 2) K1_PAIR(false, 2, 0);
      wait_lgkm0();
      bar();
      STAMP(act, 2);
    }
    {
      int l = tid & 63, wn = w & 3, wm = w >> 2, ct = ct_, rt = rt_;
      asm volatile("" : "+v"(l), "+s"(wm), "+s"(wn), "+s"(ct), "+s"(rt));
      const int g4 = l >> 4, l15 = l & 15, hf = l >> 5, l31 = l & 31;
      if (dopack) PACK_LOAD();
      float* csp = (float*)(smem + CS_OFF);
#pragma unroll
      for (int i = 0; i < 3; ++i) {
        const float m = sum8(sx[i]) * (1.0f / D_);
        const float v = fmaxf(sum8(sq[i]) * (1.0f / D_) - m * m, 0.f) + 1e-5f;
        if ((l & 7) == 0) *(f32x2*)(csp + 1024 + 2 * (24 * w + 8 * i + (l >> 3))) = f32x2{m, v};
      }
      if (l15 < 2) {
        *(f32x4*)(csp + wm * 256 + 32 * wn + 16 * l15 + 4 * g4) = cs[0];
        *(f32x4*)(csp + wm * 256 + 128 + 32 * wn + 16 * l15 + 4 * g4) = cs[1];
      }
      __syncthreads();
      float mean[3], rstd[3], vare[3];
#pragma unroll
      for (int i = 0; i < 3; ++i) {
        const f32x2 mv = *(const f32x2*)(csp + 1024 + 2 * (96 * wm + 32 * i + l31));
        mean[i] = mv[0]; vare[i] = mv[1];
        rstd[i] = __builtin_amdgcn_rsqf(mv[1]);
      }
      const float* cq = csp + 32 * wn + 4 * hf;
#define COLSUM(e, rq) (*(const f32x4*)(cq + 128 * (e) + 8 * (rq)) + *(const f32x4*)(cq + 256 + 128 * (e) + 8 * (rq)))
#define SUB4(v, rq) f32x4{(v)[4 * (rq)], (v)[4 * (rq) + 1], (v)[4 * (rq) + 2], (v)[4 * (rq) + 3]}
      if (!isA) {
        const bool colok = ct * 128 + wn * 32 < F_;
        const float c1 = -2.0f * 0.7978845608028654f * 1.4426950408889634f, c3 = c1 * 0.044715f;
        const int ks = 20 + 4 * ct + wn;
#pragma unroll
        for (int half = 0; half < 2; ++half) {
          f32x4 cg[2], cu[2];
#pragma unroll
          for (int a = 0; a < 2; ++a) { cg[a] = COLSUM(0, 2 * half + a); cu[a] = COLSUM(1, 2 * half + a); }
#pragma unroll
          for (int i = 0; i < 3; ++i) {
            const float k1 = c1 * rstd[i], k3 = c3 * rstd[i] * rstd[i] * rstd[i];
            unsigned pk[4];
#pragma unroll
            for (int a = 0; a < 2; ++a) {
              const f32x4 g = SUB4(acc[i][0], 2 * half + a) - mean[i] * cg[a];
              const f32x4 u = SUB4(acc[i][1], 2 * half + a) - mean[i] * cu[a];
              const f32x4 z = g * (g * g * k3 + k1);
              f32x4 e;
#pragma unroll
              for (int r = 0; r < 4; ++r) e[r] = __builtin_amdgcn_exp2f(z[r]);
              e = e * vare[i] + vare[i];
#pragma unroll
              for (int r = 0; r < 4; ++r) e[r] = __builtin_amdgcn_rcpf(e[r]);
              const f32x4 h = (g * u) * e;
              pk[2 * a] = pack2(h[0], h[1]); pk[2 * a + 1] = pack2(h[2], h[3]);
            }
            const int tu = 12 * rt + 6 * wm + 2 * i;
            if (colok && tu < 128)
              *(u32x4*)(act + (long)(tu * KT2 + ks) * 1024 + (l31 >> 4) * (KT2 * 1024) + (2 * half + hf) * 256 + l15 * 16) =
                  u32x4{pk[0], pk[1], pk[2], pk[3]};
          }
        }
      } else {
#pragma unroll
        for (int e = 0; e < 2; ++e)
#pragma unroll
          for (int rq = 0; rq < 4; ++rq) {
            const f32x4 c = COLSUM(e, rq);
            if (e == 0 || wn < 3 || rq < 2) {
#pragma unroll
              for (int i = 0; i < 3; ++i) {
                const f32x4 v = (SUB4(acc[i][e], rq) - mean[i] * c) * rstd[i];
                *(u32x2*)(smem + (96 * wm + 32 * i + l31) * QROW + (128 * e + 32 * wn + 8 * rq + 4 * hf) * 2) =
                    u32x2{pack2(v[0], v[1]), pack2(v[2], v[3])};
              }
            }
          }
        __syncthreads();
        const float sc = 0.22360679774997896f * 1.4426950408889634f;
        const bool lo = g4 < 3, hi = g4 < 2;
        const int dq = l15 - 4 * g4;
        const bool par = l15 & 1;
        const int dA = dq - (l15 & 1), dB = dA - 2;
        const bool curA = dA >= 0, curB = dB >= 0;
        const unsigned psh = par ? 16u : 0u;
        const int nit = rt == 11 ? 14 : 22;
#pragma nounroll
        for (int it = w; it < nit; it += 8) {
          const int iu = 1 + (it >> 1);
          const int tu = 11 * rt - 1 + iu;
          const float bA = (curA || tu > 0) ? 0.f : -3.0e38f, bB = (curB || tu > 0) ? 0.f : -3.0e38f;
          const f32x4 z4 = {0.f, 0.f, 0.f, 0.f};
          u32x2 oa[2], ob[2];
#pragma unroll
          for (int e = 0; e < 2; ++e) {
            const int hh = 2 * (it & 1) + e;
            const unsigned rq = sbase + (16 * iu + l15) * QROW + 40 * hh + 16 * g4;
            const u32x2 q0 = lds_read8(rq + 320), q1 = lds_read8(rq + 328);
            const u32x2 a0 = lds_read8(rq - 16 * QROW), a1 = lds_read8(rq - 16 * QROW + 8);
            const u32x2 b0 = lds_read8(rq), b1 = lds_read8(rq + 8);
            const unsigned rv = sbase + (16 * iu + (l >> 2)) * QROW + 160 + 40 * hh + 8 * (l & 3);
            const s16x4 v00 = tr_read(rv - 16 * QROW), v01 = tr_read(rv);
            const s16x4 v10 = tr_read(rv - 16 * QROW + 32), v11 = tr_read(rv + 32);
            const u32x4 qf = {lo ? q0[0] : 0u, lo ? q0[1] : 0u, hi ? q1[0] : 0u, hi ? q1[1] : 0u};
            const u32x4 kp = {a0[0], a0[1], a1[0], a1[1]};
            const u32x4 kc = {b0[0], b0[1], b1[0], b1[1]};
            const f32x4 s0 = MFMA(__builtin_bit_cast(bf16x8, kp), __builtin_bit_cast(bf16x8, qf), z4);
            const f32x4 s1 = MFMA(__builtin_bit_cast(bf16x8, kc), __builtin_bit_cast(bf16x8, qf), z4);
            const float sA = __builtin_fmaf(curA ? (par ? s1[1] : s1[0]) : (par ? s0[1] : s0[0]), sc, bA);
            const float sB = __builtin_fmaf(curB ? (par ? s1[3] : s1[2]) : (par ? s0[3] : s0[2]), sc, bB);
            const float m = xmax4(fmaxf(sA, sB));
            const float eA = __builtin_amdgcn_exp2f(sA - m), eB = __builtin_amdgcn_exp2f(sB - m);
            const float inv = __builtin_amdgcn_rcpf(xsum4(eA + eB));
            const unsigned pk = pack2(eA * inv, eB * inv);
            const unsigned pA = (pk & 0xffffu) << psh, pB = (pk >> 16) << psh;
            const u32x4 pf = {curA ? 0u : pA, curB ? 0u : pB, curA ? pA : 0u, curB ? pB : 0u};
            const f32x4 o0 = MFMA(cat(v00, v01), __builtin_bit_cast(bf16x8, pf), z4);
            const f32x4 o1 = MFMA(cat(v10, v11), __builtin_bit_cast(bf16x8, pf), z4);
            oa[e] = u32x2{pack2(o0[0], o0[1]), pack2(o0[2], o0[3])};
            ob[e] = u32x2{pack2(o1[0], o1[1]), pack2(o1[2], o1[3])};
          }
          {
            char* ab = act + (long)tu * (KT2 * 1024) + l15 * 16;
#pragma unroll
            for (int e = 0; e < 2; ++e) {
              const int c0 = 80 * ct + 40 * (it & 1) + 20 * e;
              const int col = c0 + 4 * g4, col2 = c0 + 16;
              *(u32x2*)(ab + (col >> 3) * 256 + (col & 4) * 2) = oa[e];
              if (g4 == 0) *(u32x2*)(ab + (col2 >> 3) * 256 + (col2 & 4) * 2) = ob[e];
            }
          }
        }
      }
    }
  }

  STAMP(act, 3);
  if (dopack) {
    if (f0 >= 154) PACK_LOAD();
    char* dst = w2p + (long)((4 * pnb + (l15 >> 2)) * KT2 + pks) * 1024 + g4 * 256 + (l15 & 3) * 64;
#pragma unroll
    for (int i = 0; i < 4; ++i) {
      const unsigned sel = (i & 1) ? 0x07060302u : 0x05040100u;
      u32x4 o;
#pragma unroll
      for (int jj = 0; jj < 4; ++jj) o[jj] = __builtin_amdgcn_perm(pv[2 * jj + 1][i >> 1], pv[2 * jj][i >> 1], sel);
      *(u32x4*)(dst + i * 16) = o;
    }
  }
  STAMP(act, 4); STAMPR(act, 9);
}

constexpr int K2D = 3;
__global__ __launch_bounds__(256, 2) void k2_kernel(const char* __restrict__ x, const char* __restrict__ act,
                                                     const char* __restrict__ w2p, char* __restrict__ out) {
  extern __shared__ __attribute__((aligned(16))) char smem[];
  const int tid = threadIdx.x, l = tid & 63;
  const int w = __builtin_amdgcn_readfirstlane(tid >> 6);
  const int b = blockIdx.x, xcd = b & 7, idx = b >> 3;
  const int rt = (xcd >> 1) * 8 + (idx >> 2), ct = (xcd & 1) * 4 + (idx & 3);
  const char* ap = act + (long)(4 * rt * KT2 + w) * 1024;
  const char* bp = w2p + (long)(5 * ct * KT2 + w) * 1024;
  const unsigned lo = l * 16;
  unsigned la[4];
#pragma unroll
  for (int i = 0; i < 4; ++i) la[i] = lo + (i ^ w) * (KT2 * 1024);
  STAMP(act + (1 << 20), 0); STAMPR(act + (1 << 20), 8);
  bf16x8 fa[K2D][4], fb[K2D][5];
  f32x4 acc[4][5];
#define K2_LOAD(d)                                                                                    \
  do {                                                                                                \
    _Pragma("unroll") for (int u = 0; u < 5; ++u) fb[d][u] = *(const bf16x8*)(bp + (lo + u * (KT2 * 1024))); \
    _Pragma("unroll") for (int i = 0; i < 4; ++i) fa[d][i] = *(const bf16x8*)(ap + la[i]); \
    ap += 4096; bp += 4096;                                                                           \
  } while (0)
#define K2_MMA(d)                                                                                     \
  do {                                                                                                \
    _Pragma("unroll") for (int u = 0; u < 5; ++u)                                                     \
    _Pragma("unroll") for (int i = 0; i < 4; ++i)                                                     \
      acc[i][u] = __builtin_amdgcn_mfma_f32_16x16x32_bf16(fb[d][u], fa[d][i], acc[i][u], 0, 0, 0);    \
  } while (0)
#pragma unroll
  for (int d = 0; d < K2D; ++d) K2_LOAD(d);
#pragma unroll
  for (int i = 0; i < 4; ++i)
#pragma unroll
    for (int u = 0; u < 5; ++u) acc[i][u] = f32x4{0.f, 0.f, 0.f, 0.f};
  const int g4 = l >> 4, l15 = l & 15;
  const unsigned ooff = (16 * (4 * rt + w) + l15) * 1280 + (80 * ct + 4 * g4) * 2;
  __builtin_amdgcn_sched_barrier(0);
  STAMP(act + (1 << 20), 1);
#pragma nounroll
  for (int r = 0; r < 5; ++r) {
#pragma unroll
    for (int d = 0; d < K2D; ++d) {
      K2_MMA(d);
      K2_LOAD(d);
      __builtin_amdgcn_sched_barrier(0);
    }
  }
  STAMP(act + (1 << 20), 2);
  K2_MMA(0);
  if (w < 2) K2_LOAD(0);
  u32x2 xr[5];
#pragma unroll
  for (int u = 0; u < 5; ++u) xr[u] = *(const u32x2*)(x + (ooff + u * 32));
  __builtin_amdgcn_sched_barrier(0);
  K2_MMA(1);
  K2_MMA(2);
  if (w < 2) K2_MMA(0);
  STAMP(act + (1 << 20), 3);
  f32x4* red = (f32x4*)smem;
#pragma unroll
  for (int i = 1; i < 4; ++i)
#pragma unroll
    for (int u = 0; u < 5; ++u) red[(((i ^ w) * 3 + i - 1) * 5 + u) * 64 + l] = acc[i][u];
  __syncthreads();
#pragma unroll
  for (int u = 0; u < 5; ++u) {
    const f32x4 s = (acc[0][u] + red[((w * 3 + 0) * 5 + u) * 64 + l]) +
                    (red[((w * 3 + 1) * 5 + u) * 64 + l] + red[((w * 3 + 2) * 5 + u) * 64 + l]);
    const float x0 = __builtin_bit_cast(float, xr[u][0] << 16), x1 = __builtin_bit_cast(float, xr[u][0] & 0xffff0000u);
    const float x2 = __builtin_bit_cast(float, xr[u][1] << 16), x3 = __builtin_bit_cast(float, xr[u][1] & 0xffff0000u);
    u32x2 o = {pack2(x0 + s[0], x1 + s[1]), pack2(x2 + s[2], x3 + s[3])};
    *(u32x2*)(out + (ooff + u * 32)) = o;
  }
  STAMP(act + (1 << 20), 4); STAMPR(act + (1 << 20), 9);
}

extern "C" void kernel_launch(void* const* d_in, const int* in_sizes, int n_in, void* d_out, int out_size,
                              void* d_ws, size_t ws_size, hipStream_t stream) {
  const char* x = (const char*)d_in[0];
  const char* wqkv = (const char*)d_in[1];
  const char* wo = (const char*)d_in[2];
  const char* wg = (const char*)d_in[4];
  const char* wu = (const char*)d_in[5];
  const char* wd = (const char*)d_in[6];
  char* act = (char*)d_ws;
  char* w2p = act + (size_t)128 * KT2 * 1024;
  (void)hipFuncSetAttribute((const void*)k1_kernel, hipFuncAttributeMaxDynamicSharedMemorySize, LDS1);
  (void)hipFuncSetAttribute((const void*)k2_kernel, hipFuncAttributeMaxDynamicSharedMemorySize, 61440);
  k1_kernel<<<256, 512, LDS1, stream>>>(x, wqkv, wo, wg, wu, wd, act, w2p);
  k2_kernel<<<256, 256, 61440, stream>>>(x, act, w2p, (char*)d_out);
}
```

```cpp
#include <hip/hip_runtime.h>
#include <hip/hip_bf16.h>
#include <stdint.h>

typedef __attribute__((ext_vector_type(8))) short bf16x8;
typedef __attribute__((ext_vector_type(4))) short s16x4;
typedef __attribute__((ext_vector_type(4))) float f32x4;
typedef __attribute__((ext_vector_type(2))) float f32x2;
typedef __attribute__((ext_vector_type(16))) float f32x16;
typedef __attribute__((ext_vector_type(2))) __bf16 bf2;
typedef __attribute__((ext_vector_type(4))) unsigned u32x4;
typedef __attribute__((ext_vector_type(2))) unsigned u32x2;
#define LDS_AS __attribute__((address_space(3)))
#define STAMP(base, i)
#define STAMPR(base, i)

constexpr int S_ = 2048, D_ = 640, F_ = 1728;
constexpr int KT2 = 74;
constexpr int NS = 20;
constexpr int WSLOT = 16384, XSLOT = 24576;
constexpr int W_OFF = 0, X_OFF = 5 * WSLOT;
constexpr int LDS1 = 5 * WSLOT + 3 * XSLOT;
constexpr int QROW = 488;
constexpr int CS_OFF = 192 * QROW;

__device__ __forceinline__ float dot2(unsigned a, unsigned b, float c) {
  return __builtin_amdgcn_fdot2_f32_bf16(__builtin_bit_cast(bf2, a), __builtin_bit_cast(bf2, b), c, false);
}
__device__ __forceinline__ unsigned pack2(float a, float b) {
  f32x2 p = {a, b};
  return __builtin_bit_cast(unsigned, __builtin_convertvector(p, bf2));
}
__device__ __forceinline__ void glds16(const char* base, unsigned voff, unsigned lds_dst, unsigned lds_off) {
  asm volatile("s_add_u32 m0, %2, %3\n\ts_nop 0\n\tglobal_load_lds_dwordx4 %0, %1" ::"v"(voff), "s"(base), "s"(lds_dst), "s"(lds_off)
               : "memory", "scc");
}
__device__ __forceinline__ s16x4 tr_read(unsigned lds_off) {
  return __builtin_amdgcn_ds_read_tr16_b64_v4i16((LDS_AS s16x4*)(uintptr_t)lds_off);
}
__device__ __forceinline__ bf16x8 lds_read16(unsigned lds_off) { return *(LDS_AS bf16x8*)(uintptr_t)lds_off; }
__device__ __forceinline__ u32x2 lds_read8(unsigned lds_off) { return *(LDS_AS u32x2*)(uintptr_t)lds_off; }
__device__ __forceinline__ bf16x8 cat(s16x4 a, s16x4 b) { return __builtin_shufflevector(a, b, 0, 1, 2, 3, 4, 5, 6, 7); }
template <int N> __device__ __forceinline__ void wait_vm() { asm volatile("s_waitcnt vmcnt(%0)" ::"n"(N) : "memory"); }
__device__ __forceinline__ void wait_lgkm0() { asm volatile("s_waitcnt lgkmcnt(0)" ::: "memory"); }
__device__ __forceinline__ void bar() { __builtin_amdgcn_s_barrier(); }
#define MFMA(a, b, c) __builtin_amdgcn_mfma_f32_16x16x32_bf16(a, b, c, 0, 0, 0)

__device__ __forceinline__ void swap16(float v, float& a, float& b) {
  a = v; b = v;
  asm volatile("s_nop 1\n\tv_permlane16_swap_b32 %0, %1" : "+v"(a), "+v"(b));
}
__device__ __forceinline__ void swap32(float v, float& a, float& b) {
  a = v; b = v;
  asm volatile("s_nop 1\n\tv_permlane32_swap_b32 %0, %1" : "+v"(a), "+v"(b));
}
__device__ __forceinline__ float xsum4(float v) {
  float a, b;
  swap16(v, a, b); v = a + b;
  swap32(v, a, b); return a + b;
}
__device__ __forceinline__ float xmax4(float v) {
  float a, b;
  swap16(v, a, b); v = fmaxf(a, b);
  swap32(v, a, b); return fmaxf(a, b);
}
__device__ __forceinline__ float swap1(float v) {
  return v + __builtin_bit_cast(float, __builtin_amdgcn_update_dpp(0, __builtin_bit_cast(int, v), 0xB1, 0xF, 0xF, true));
}
#define MFMA32(a, b, c) __builtin_amdgcn_mfma_f32_32x32x16_bf16(a, b, c, 0, 0, 0)
#define SB() __builtin_amdgcn_sched_barrier(0)
#define RDW(base, e) cat(tr_read((base) + (e) * 8192), tr_read((base) + (e) * 8192 + 1024))
#define STAT(d) do {                                                                                     \
    if ((d) == 0 || (d) == 4 || (d) == 9) { SB(); sq[(d) >> 2] = MFMA(sv[(d) >> 2], sv[(d) >> 2], sq[(d) >> 2]); }   \
    if ((d) == 2 || (d) == 7 || (d) == 11) { SB(); sx[(d) >> 2] = MFMA(sel, sv[(d) >> 2], sx[(d) >> 2]); }           \
  } while (0)
#define CSUM(e) do { SB(); cs[e] = MFMA(wa[e], sel, cs[e]); } while (0)
#define K1_STEP(EVEN, MORE)                                                                              \
  do {                                                                                                   \
                 \
    const unsigned xh = xb ^ 32u;                                                                        \
    acc[0][0] = MFMA32(wa[0], xa[0], acc[0][0]); wc[0] = RDW(wb1, 0); xc[0] = lds_read16(xh); SB();       \
    acc[1][0] = MFMA32(wa[0], xa[1], acc[1][0]); xc[1] = lds_read16(xh + 4096); xc[2] = lds_read16(xh + 8192); \
    if (EVEN) sv[0] = lds_read16(sb_); SB();                                   \
    acc[2][0] = MFMA32(wa[0], xa[2], acc[2][0]); wc[1] = RDW(wb1, 1); CSUM(0); SB();                   \
    acc[0][1] = MFMA32(wa[1], xa[0], acc[0][1]);                                              \
    if (EVEN) { sv[1] = lds_read16(sb_ + 1024); sv[2] = lds_read16(sb_ + 2048); } \
    else STAT(6);                                                                                        \
    SB();                                                                                                \
    acc[1][1] = MFMA32(wa[1], xa[1], acc[1][1]); if (MORE) glds16(ws0, wo0, wdst, wfill); SB(); \
    acc[2][1] = MFMA32(wa[1], xa[2], acc[2][1]); if (MORE) glds16(ws1, wo1, wdst + 8192, wfill); CSUM(1); \
    wb0 += winc; wb1 += winc; if (EVEN) xb ^= 64u; else { xb = (xb ^ 64u) + xinc; sb_ += xinc; } SB();    \
                      \
    acc[0][0] = MFMA32(wc[0], xc[0], acc[0][0]); wa[0] = RDW(wb0, 0); xa[0] = lds_read16(xb); SB();       \
    acc[1][0] = MFMA32(wc[0], xc[1], acc[1][0]); xa[1] = lds_read16(xb + 4096); xa[2] = lds_read16(xb + 8192); \
    STAT(EVEN ? 0 : 7); SB();                                                                            \
    acc[2][0] = MFMA32(wc[0], xc[2], acc[2][0]); wa[1] = RDW(wb0, 1); STAT(EVEN ? 1 : 8); SB();           \
    acc[0][1] = MFMA32(wc[1], xc[0], acc[0][1]); STAT(EVEN ? 2 : 9);                                      \
    if (EVEN && MORE) glds16(xs, xo[0], xdst, xfill); SB();                                               \
    acc[1][1] = MFMA32(wc[1], xc[1], acc[1][1]); STAT(EVEN ? 3 : 10);                                     \
    if (EVEN && MORE) glds16(xs, xo[1], xdst + 1024, xfill); SB();                                        \
    acc[2][1] = MFMA32(wc[1], xc[2], acc[2][1]); STAT(EVEN ? 4 : 11);                                     \
    if (EVEN && MORE) glds16(xs, xo[2], xdst + 2048, xfill);                                              \
    if (EVEN) STAT(5);                                                                                   \
    SB();                                                                                                \
  } while (0)
#define K1_PAIR(MORE, W0, W1)                                                                            \
  do {                                                                                                   \
    int winc = wfill == 3 * WSLOT ? -4 * WSLOT : WSLOT;                                                  \
    const int xinc = xfill == XSLOT ? -2 * XSLOT : XSLOT;                                                \
    K1_STEP(true, MORE);                                                                                 \
    ws0 += wstep; ws1 += wstep; xs += 128;                                                               \
    wfill = wfill == 4 * WSLOT ? 0 : wfill + WSLOT;                                                      \
    xfill = xfill == 2 * XSLOT ? 0 : xfill + XSLOT;                                                      \
    winc = wfill == 3 * WSLOT ? -4 * WSLOT : WSLOT;                                                      \
    wait_vm<W0>();                                                                                       \
    bar();                                                                                               \
    K1_STEP(false, MORE);                                                                                \
    ws0 += wstep; ws1 += wstep;                                                                          \
    wfill = wfill == 4 * WSLOT ? 0 : wfill + WSLOT;                                                      \
    wait_vm<W1>();                                                                                       \
    bar();                                                                                               \
  } while (0)

__global__ __launch_bounds__(512, 2) void k1_kernel(const char* __restrict__ x, const char* __restrict__ wqkv,
                                                     const char* __restrict__ wo, const char* __restrict__ wg,
                                                     const char* __restrict__ wu, const char* __restrict__ wd,
                                                     char* __restrict__ act, char* __restrict__ w2p) {
  extern __shared__ __attribute__((aligned(16))) char smem[];
  const int tid = threadIdx.x, l = tid & 63;
  const int w = __builtin_amdgcn_readfirstlane(tid >> 6);
  const int wn = w & 3, wm = w >> 2;
  const int b = blockIdx.x, xcd = b & 7, idx = b >> 3;
  const bool isA = xcd < 3;
  const int lin = xcd * 32 + idx, f0 = lin - 96;
  const int fh = f0 >= 84, f1 = fh ? f0 - 84 : f0;
  const int fct = fh ? f1 / 5 : f1 / 6;
  const int ct = isA ? (lin & 7) : fct;
  const int rt = isA ? (lin >> 3) : (fh ? 6 + f1 - fct * 5 : f1 - fct * 6);
  const int ct_ = ct, rt_ = rt;
  STAMP(act, 0); STAMPR(act, 8);
  const int g4 = l >> 4, l15 = l & 15;
  const unsigned sbase = (unsigned)(uintptr_t)(LDS_AS char*)smem;

  const int psb = f0 * 5 + w;
  const bool dopack = !isA && w < 5 && psb < 740;
  const int pks = psb / 10, pnb = psb - pks * 10;
  u32x2 pv[8];
#define PACK_LOAD()                                                                                                     \
  do {                                                                                                                  \
    const char* p_ = (pks < 20 ? wo + (long)(32 * pks) * 1280 : wd + (long)(32 * (pks - 20)) * 1280) + 128 * pnb;       \
    const unsigned ro = (pks < 20 ? 8 * g4 : 16 * (g4 >> 1) + 4 * (g4 & 1)) * 1280 + 8 * l15;                           \
    const unsigned r4 = ro + (pks < 20 ? 4 : 8) * 1280;                                                                 \
    _Pragma("unroll") for (int j = 0; j < 8; ++j) pv[j] = *(const u32x2*)(p_ + ((j < 4 ? ro : r4) + (j & 3) * 1280));   \
  } while (0)

  if (f0 < 154) {
    const int t0 = isA ? 176 * rt - 16 : 192 * rt;
    unsigned xo[3];
#pragma unroll
    for (int i = 0; i < 3; ++i) {
      const int row = 24 * w + 8 * i + (l >> 3);
      int gr = t0 + row;
      gr = gr < 0 ? 0 : (gr > S_ - 1 ? S_ - 1 : gr);
      xo[i] = gr * 1280 + (((l & 7) ^ ((row >> 1) & 7)) << 4);
    }
    const unsigned xdst = sbase + X_OFF + w * 3072, wdst = sbase + W_OFF + w * 1024;
    const char* xs = x;
#define ISSUE_X(slot_off) do { glds16(xs, xo[0], xdst, slot_off); glds16(xs, xo[1], xdst + 1024, slot_off); \
                               glds16(xs, xo[2], xdst + 2048, slot_off); xs += 128; } while (0)
    ISSUE_X(0);
    const int wstride = isA ? 3840 : 3456;
    unsigned wo0, wo1;
    {
      const int c16 = l15 ^ (g4 << 2);
      const int rowo = (4 * w + g4) * wstride;
      const int a0 = c16 * 16 + (c16 < 10 ? 1280 : 2400) + ct * 160;
      const int a1 = (c16 < 14 ? c16 : c16 - 2) * 16 + (c16 < 4 ? 2656 : -64) + ct * 160;
      const int fc = ct * 256 + c16 * 16;
      const int f = fc >= 2 * F_ ? fc - 128 : fc;
      wo0 = rowo + (isA ? a0 : f);
      wo1 = rowo + (isA ? a1 : f);
    }
    const char *ws0 = isA ? wqkv : wg, *ws1 = isA ? wqkv : wu;
    const int wstep = 32 * wstride;
#define ISSUE_W(slot_off) do { glds16(ws0, wo0, wdst, slot_off); glds16(ws1, wo1, wdst + 8192, slot_off); \
                               ws0 += wstep; ws1 += wstep; } while (0)
    ISSUE_W(0); ISSUE_W(WSLOT);
    const int hf = l >> 5, l31 = l & 31;
    unsigned xb = sbase + X_OFF + (96 * wm + l31) * 128 + 16 * (unsigned)((2 * wm + hf) ^ ((l31 >> 1) & 7));
    unsigned wb0, wb1;
    {
      const int q = l15 >> 2;
      const unsigned wbase = sbase + W_OFF + (8 * hf + q) * 256 + 64 * (wn ^ q) + 32 * (g4 & 1) + 8 * (l & 3);
      wb0 = wbase + wm * 4096;
      wb1 = wbase + (1 - wm) * 4096;
    }
    f32x16 acc[3][2];
    f32x4 sx[3], sq[3];
    f32x4 cs[2];
#pragma unroll
    for (int i = 0; i < 3; ++i) {
      sx[i] = sq[i] = f32x4{0.f, 0.f, 0.f, 0.f};
#pragma unroll
      for (int r = 0; r < 16; ++r) { acc[i][0][r] = 0.f; acc[i][1][r] = 0.f; }
    }
    cs[0] = cs[1] = f32x4{0.f, 0.f, 0.f, 0.f};
    const unsigned s1_ = l15 == (g4 & 1) ? 0x3f803f80u : 0u;
    u32x4 sel_ = {s1_, s1_, s1_, s1_};
    asm volatile("" : "+v"(sel_));
    const bf16x8 sel = __builtin_bit_cast(bf16x8, sel_);
    unsigned sb_ = xdst + (l15 >> 1) * 128 + 16 * ((2 * g4 + (l15 & 1)) ^ (2 * (l15 >> 2)));
    ISSUE_X(XSLOT); ISSUE_W(2 * WSLOT);
    {
      bf16x8 xa[3], xc[3], wa[2], wc[2];
      bf16x8 sv[3];
      wait_vm<7>();
      bar();
      STAMP(act, 1);
      ISSUE_W(3 * WSLOT);
#pragma unroll
      for (int i = 0; i < 3; ++i) xa[i] = lds_read16(xb + i * 4096);
      wa[0] = RDW(wb0, 0); wa[1] = RDW(wb0, 1);
      wait_vm<7>();
      bar();
      int wfill = 4 * WSLOT, xfill = 2 * XSLOT;
#pragma nounroll
      for (int s = 0; s < 16; s += 2) K1_PAIR(true, 7, 7);
#pragma nounroll
      for (int s = 16; s < NS; s += 2) K1_PAIR(false, 2, 0);
      wait_lgkm0();
      bar();
      STAMP(act, 2);
    }
    {
      int l = tid & 63, wn = w & 3, wm = w >> 2, ct = ct_, rt = rt_;
      asm volatile("" : "+v"(l), "+s"(wm), "+s"(wn), "+s"(ct), "+s"(rt));
      const int g4 = l >> 4, l15 = l & 15, hf = l >> 5, l31 = l & 31;
      if (dopack) PACK_LOAD();
      float* csp = (float*)(smem + CS_OFF);
#pragma unroll
      for (int i = 0; i < 3; ++i) {
        const float a = swap1(sx[i][0] + sx[i][1]);
        const float q = swap1((l15 & 2) ? ((l15 & 1) ? sq[i][3] : sq[i][2]) : ((l15 & 1) ? sq[i][1] : sq[i][0]));
        float* sp = csp + 1024 + 2 * (24 * w + 8 * i + (l15 >> 1));
        if (!(l15 & 1) && g4 == 0) sp[0] = a;
        if (!(l15 & 1) && g4 == (l15 >> 2)) sp[1] = q;
      }
      if (l15 < 2) {
        *(f32x4*)(csp + wm * 256 + 32 * wn + 16 * l15 + 4 * g4) = cs[0];
        *(f32x4*)(csp + wm * 256 + 128 + 32 * wn + 16 * l15 + 4 * g4) = cs[1];
      }
      __syncthreads();
      float mean[3], rstd[3], vare[3];
#pragma unroll
      for (int i = 0; i < 3; ++i) {
        const f32x2 mv = *(const f32x2*)(csp + 1024 + 2 * (96 * wm + 32 * i + l31));
        mean[i] = mv[0] * (1.0f / D_);
        vare[i] = fmaxf(mv[1] * (1.0f / D_) - mean[i] * mean[i], 0.f) + 1e-5f;
        rstd[i] = __builtin_amdgcn_rsqf(vare[i]);
      }
      const float* cq = csp + 32 * wn + 4 * hf;
#define COLSUM(e, rq) (*(const f32x4*)(cq + 128 * (e) + 8 * (rq)) + *(const f32x4*)(cq + 256 + 128 * (e) + 8 * (rq)))
#define SUB4(v, rq) f32x4{(v)[4 * (rq)], (v)[4 * (rq) + 1], (v)[4 * (rq) + 2], (v)[4 * (rq) + 3]}
      if (!isA) {
        const bool colok = ct * 128 + wn * 32 < F_;
        const float c1 = -2.0f * 0.7978845608028654f * 1.4426950408889634f, c3 = c1 * 0.044715f;
        const int ks = 20 + 4 * ct + wn;
#pragma unroll
        for (int half = 0; half < 2; ++half) {
          f32x4 cg[2], cu[2];
#pragma unroll
          for (int a = 0; a < 2; ++a) { cg[a] = COLSUM(0, 2 * half + a); cu[a] = COLSUM(1, 2 * half + a); }
#pragma unroll
          for (int i = 0; i < 3; ++i) {
            const float k1 = c1 * rstd[i], k3 = c3 * rstd[i] * rstd[i] * rstd[i];
            unsigned pk[4];
#pragma unroll
            for (int a = 0; a < 2; ++a) {
              const f32x4 g = SUB4(acc[i][0], 2 * half + a) - mean[i] * cg[a];
              const f32x4 u = SUB4(acc[i][1], 2 * half + a) - mean[i] * cu[a];
              const f32x4 z = g * (g * g * k3 + k1);
              f32x4 e;
#pragma unroll
              for (int r = 0; r < 4; ++r) e[r] = __builtin_amdgcn_exp2f(z[r]);
              e = e * vare[i] + vare[i];
#pragma unroll
              for (int r = 0; r < 4; ++r) e[r] = __builtin_amdgcn_rcpf(e[r]);
              const f32x4 h = (g * u) * e;
              pk[2 * a] = pack2(h[0], h[1]); pk[2 * a + 1] = pack2(h[2], h[3]);
            }
            const int tu = 12 * rt + 6 * wm + 2 * i;
            if (colok && tu < 128)
              *(u32x4*)(act + (long)(tu * KT2 + ks) * 1024 + (l31 >> 4) * (KT2 * 1024) + (2 * half + hf) * 256 + l15 * 16) =
                  u32x4{pk[0], pk[1], pk[2], pk[3]};
          }
        }
      } else {
#pragma unroll
        for (int e = 0; e < 2; ++e)
#pragma unroll
          for (int rq = 0; rq < 4; ++rq) {
            const f32x4 c = COLSUM(e, rq);
            if (e == 0 || wn < 3 || rq < 2) {
#pragma unroll
              for (int i = 0; i < 3; ++i) {
                const f32x4 v = (SUB4(acc[i][e], rq) - mean[i] * c) * rstd[i];
                *(u32x2*)(smem + (96 * wm + 32 * i + l31) * QROW + (128 * e + 32 * wn + 8 * rq + 4 * hf) * 2) =
                    u32x2{pack2(v[0], v[1]), pack2(v[2], v[3])};
              }
            }
          }
        __syncthreads();
        const float sc = 0.22360679774997896f * 1.4426950408889634f;
        const bool lo = g4 < 3, hi = g4 < 2;
        const int dq = l15 - 4 * g4;
        const bool par = l15 & 1;
        const int dA = dq - (l15 & 1), dB = dA - 2;
        const bool curA = dA >= 0, curB = dB >= 0;
        const unsigned psh = par ? 16u : 0u;
        const int nit = rt == 11 ? 14 : 22;
#pragma nounroll
        for (int it = w; it < nit; it += 8) {
          const int iu = 1 + (it >> 1);
          const int tu = 11 * rt - 1 + iu;
          const float bA = (curA || tu > 0) ? 0.f : -3.0e38f, bB = (curB || tu > 0) ? 0.f : -3.0e38f;
          const f32x4 z4 = {0.f, 0.f, 0.f, 0.f};
          u32x2 oa[2], ob[2];
#pragma unroll
          for (int e = 0; e < 2; ++e) {
            const int hh = 2 * (it & 1) + e;
            const unsigned rq = sbase + (16 * iu + l15) * QROW + 40 * hh + 16 * g4;
            const u32x2 q0 = lds_read8(rq + 320), q1 = lds_read8(rq + 328);
            const u32x2 a0 = lds_read8(rq - 16 * QROW), a1 = lds_read8(rq - 16 * QROW + 8);
            const u32x2 b0 = lds_read8(rq), b1 = lds_read8(rq + 8);
            const unsigned rv = sbase + (16 * iu + (l >> 2)) * QROW + 160 + 40 * hh + 8 * (l & 3);
            const s16x4 v00 = tr_read(rv - 16 * QROW), v01 = tr_read(rv);
            const s16x4 v10 = tr_read(rv - 16 * QROW + 32), v11 = tr_read(rv + 32);
            const u32x4 qf = {lo ? q0[0] : 0u, lo ? q0[1] : 0u, hi ? q1[0] : 0u, hi ? q1[1] : 0u};
            const u32x4 kp = {a0[0], a0[1], a1[0], a1[1]};
            const u32x4 kc = {b0[0], b0[1], b1[0], b1[1]};
            const f32x4 s0 = MFMA(__builtin_bit_cast(bf16x8, kp), __builtin_bit_cast(bf16x8, qf), z4);
            const f32x4 s1 = MFMA(__builtin_bit_cast(bf16x8, kc), __builtin_bit_cast(bf16x8, qf), z4);
            const float sA = __builtin_fmaf(curA ? (par ? s1[1] : s1[0]) : (par ? s0[1] : s0[0]), sc, bA);
            const float sB = __builtin_fmaf(curB ? (par ? s1[3] : s1[2]) : (par ? s0[3] : s0[2]), sc, bB);
            const float m = xmax4(fmaxf(sA, sB));
            const float eA = __builtin_amdgcn_exp2f(sA - m), eB = __builtin_amdgcn_exp2f(sB - m);
            const float inv = __builtin_amdgcn_rcpf(xsum4(eA + eB));
            const unsigned pk = pack2(eA * inv, eB * inv);
            const unsigned pA = (pk & 0xffffu) << psh, pB = (pk >> 16) << psh;
            const u32x4 pf = {curA ? 0u : pA, curB ? 0u : pB, curA ? pA : 0u, curB ? pB : 0u};
            const f32x4 o0 = MFMA(cat(v00, v01), __builtin_bit_cast(bf16x8, pf), z4);
            const f32x4 o1 = MFMA(cat(v10, v11), __builtin_bit_cast(bf16x8, pf), z4);
            oa[e] = u32x2{pack2(o0[0], o0[1]), pack2(o0[2], o0[3])};
            ob[e] = u32x2{pack2(o1[0], o1[1]), pack2(o1[2], o1[3])};
          }
          {
            char* ab = act + (long)tu * (KT2 * 1024) + l15 * 16;
#pragma unroll
            for (int e = 0; e < 2; ++e) {
              const int c0 = 80 * ct + 40 * (it & 1) + 20 * e;
              const int col = c0 + 4 * g4, col2 = c0 + 16;
              *(u32x2*)(ab + (col >> 3) * 256 + (col & 4) * 2) = oa[e];
              if (g4 == 0) *(u32x2*)(ab + (col2 >> 3) * 256 + (col2 & 4) * 2) = ob[e];
            }
          }
        }
      }
    }
  }

  STAMP(act, 3);
  if (dopack) {
    if (f0 >= 154) PACK_LOAD();
    char* dst = w2p + (long)((4 * pnb + (l15 >> 2)) * KT2 + pks) * 1024 + g4 * 256 + (l15 & 3) * 64;
#pragma unroll
    for (int i = 0; i < 4; ++i) {
      const unsigned sel = (i & 1) ? 0x07060302u : 0x05040100u;
      u32x4 o;
#pragma unroll
      for (int jj = 0; jj < 4; ++jj) o[jj] = __builtin_amdgcn_perm(pv[2 * jj + 1][i >> 1], pv[2 * jj][i >> 1], sel);
      *(u32x4*)(dst + i * 16) = o;
    }
  }
  STAMP(act, 4); STAMPR(act, 9);
}

constexpr int K2D = 3;
__global__ __launch_bounds__(256, 2) void k2_kernel(const char* __restrict__ x, const char* __restrict__ act,
                                                     const char* __restrict__ w2p, char* __restrict__ out) {
  extern __shared__ __attribute__((aligned(16))) char smem[];
  const int tid = threadIdx.x, l = tid & 63;
  const int w = __builtin_amdgcn_readfirstlane(tid >> 6);
  const int b = blockIdx.x, xcd = b & 7, idx = b >> 3;
  const int rt = (xcd >> 1) * 8 + (idx >> 2), ct = (xcd & 1) * 4 + (idx & 3);
  const char* ap = act + (long)(4 * rt * KT2 + w) * 1024;
  const char* bp = w2p + (long)(5 * ct * KT2 + w) * 1024;
  const unsigned lo = l * 16;
  unsigned la[4];
#pragma unroll
  for (int i = 0; i < 4; ++i) la[i] = lo + (i ^ w) * (KT2 * 1024);
  STAMP(act + (1 << 20), 0); STAMPR(act + (1 << 20), 8);
  bf16x8 fa[K2D][4], fb[K2D][5];
  f32x4 acc[4][5];
#define K2_LOAD(d)                                                                                    \
  do {                                                                                                \
    _Pragma("unroll") for (int u = 0; u < 5; ++u) fb[d][u] = *(const bf16x8*)(bp + (lo + u * (KT2 * 1024))); \
    _Pragma("unroll") for (int i = 0; i < 4; ++i) fa[d][i] = *(const bf16x8*)(ap + la[i]); \
    ap += 4096; bp += 4096;                                                                           \
  } while (0)
#define K2_MMA(d)                                                                                     \
  do {                                                                                                \
    _Pragma("unroll") for (int u = 0; u < 5; ++u)                                                     \
    _Pragma("unroll") for (int i = 0; i < 4; ++i)                                                     \
      acc[i][u] = __builtin_amdgcn_mfma_f32_16x16x32_bf16(fb[d][u], fa[d][i], acc[i][u], 0, 0, 0);    \
  } while (0)
#pragma unroll
  for (int d = 0; d < K2D; ++d) K2_LOAD(d);
#pragma unroll
  for (int i = 0; i < 4; ++i)
#pragma unroll
    for (int u = 0; u < 5; ++u) acc[i][u] = f32x4{0.f, 0.f, 0.f, 0.f};
  const int g4 = l >> 4, l15 = l & 15;
  const unsigned ooff = (16 * (4 * rt + w) + l15) * 1280 + (80 * ct + 4 * g4) * 2;
  __builtin_amdgcn_sched_barrier(0);
  STAMP(act + (1 << 20), 1);
#pragma nounroll
  for (int r = 0; r < 5; ++r) {
#pragma unroll
    for (int d = 0; d < K2D; ++d) {
      K2_MMA(d);
      K2_LOAD(d);
      __builtin_amdgcn_sched_barrier(0);
    }
  }
  STAMP(act + (1 << 20), 2);
  K2_MMA(0);
  if (w < 2) K2_LOAD(0);
  u32x2 xr[5];
#pragma unroll
  for (int u = 0; u < 5; ++u) xr[u] = *(const u32x2*)(x + (ooff + u * 32));
  __builtin_amdgcn_sched_barrier(0);
  K2_MMA(1);
  K2_MMA(2);
  if (w < 2) K2_MMA(0);
  STAMP(act + (1 << 20), 3);
  f32x4* red = (f32x4*)smem;
#pragma unroll
  for (int i = 1; i < 4; ++i)
#pragma unroll
    for (int u = 0; u < 5; ++u) red[(((i ^ w) * 3 + i - 1) * 5 + u) * 64 + l] = acc[i][u];
  __syncthreads();
#pragma unroll
  for (int u = 0; u < 5; ++u) {
    const f32x4 s = (acc[0][u] + red[((w * 3 + 0) * 5 + u) * 64 + l]) +
                    (red[((w * 3 + 1) * 5 + u) * 64 + l] + red[((w * 3 + 2) * 5 + u) * 64 + l]);
    const float x0 = __builtin_bit_cast(float, xr[u][0] << 16), x1 = __builtin_bit_cast(float, xr[u][0] & 0xffff0000u);
    const float x2 = __builtin_bit_cast(float, xr[u][1] << 16), x3 = __builtin_bit_cast(float, xr[u][1] & 0xffff0000u);
    u32x2 o = {pack2(x0 + s[0], x1 + s[1]), pack2(x2 + s[2], x3 + s[3])};
    *(u32x2*)(out + (ooff + u * 32)) = o;
  }
  STAMP(act + (1 << 20), 4); STAMPR(act + (1 << 20), 9);
}

extern "C" void kernel_launch(void* const* d_in, const int* in_sizes, int n_in, void* d_out, int out_size,
                              void* d_ws, size_t ws_size, hipStream_t stream) {
  const char* x = (const char*)d_in[0];
  const char* wqkv = (const char*)d_in[1];
  const char* wo = (const char*)d_in[2];
  const char* wg = (const char*)d_in[4];
  const char* wu = (const char*)d_in[5];
  const char* wd = (const char*)d_in[6];
  char* act = (char*)d_ws;
  char* w2p = act + (size_t)128 * KT2 * 1024;
  (void)hipFuncSetAttribute((const void*)k1_kernel, hipFuncAttributeMaxDynamicSharedMemorySize, LDS1);
  (void)hipFuncSetAttribute((const void*)k2_kernel, hipFuncAttributeMaxDynamicSharedMemorySize, 61440);
  k1_kernel<<<256, 512, LDS1, stream>>>(x, wqkv, wo, wg, wu, wd, act, w2p);
  k2_kernel<<<256, 256, 61440, stream>>>(x, act, w2p, (char*)d_out);
}
```

```cpp
#include <hip/hip_runtime.h>
#include <hip/hip_bf16.h>
#include <stdint.h>

typedef __attribute__((ext_vector_type(8))) short bf16x8;
typedef __attribute__((ext_vector_type(4))) short s16x4;
typedef __attribute__((ext_vector_type(4))) float f32x4;
typedef __attribute__((ext_vector_type(2))) float f32x2;
typedef __attribute__((ext_vector_type(16))) float f32x16;
typedef __attribute__((ext_vector_type(2))) __bf16 bf2;
typedef __attribute__((ext_vector_type(4))) unsigned u32x4;
typedef __attribute__((ext_vector_type(2))) unsigned u32x2;
#define LDS_AS __attribute__((address_space(3)))
#define STAMP(base, i)
#define STAMPR(base, i)

constexpr int S_ = 2048, D_ = 640, F_ = 1728;
constexpr int KT2 = 74;
constexpr int NS = 20;
constexpr int WSLOT = 16384, XSLOT = 24576;
constexpr int W_OFF = 0, X_OFF = 5 * WSLOT;
constexpr int LDS1 = 5 * WSLOT + 3 * XSLOT;
constexpr int QROW = 488;
constexpr int CS_OFF = 192 * QROW;

__device__ __forceinline__ float dot2(unsigned a, unsigned b, float c) {
  return __builtin_amdgcn_fdot2_f32_bf16(__builtin_bit_cast(bf2, a), __builtin_bit_cast(bf2, b), c, false);
}
__device__ __forceinline__ unsigned pack2(float a, float b) {
  f32x2 p = {a, b};
  return __builtin_bit_cast(unsigned, __builtin_convertvector(p, bf2));
}
__device__ __forceinline__ void glds16(const char* base, unsigned voff, unsigned lds_dst, unsigned lds_off) {
  asm volatile("s_add_u32 m0, %2, %3\n\ts_nop 0\n\tglobal_load_lds_dwordx4 %0, %1" ::"v"(voff), "s"(base), "s"(lds_dst), "s"(lds_off)
               : "memory", "scc");
}
__device__ __forceinline__ s16x4 tr_read(unsigned lds_off) {
  return __builtin_amdgcn_ds_read_tr16_b64_v4i16((LDS_AS s16x4*)(uintptr_t)lds_off);
}
__device__ __forceinline__ bf16x8 lds_read16(unsigned lds_off) { return *(LDS_AS bf16x8*)(uintptr_t)lds_off; }
__device__ __forceinline__ u32x2 lds_read8(unsigned lds_off) { return *(LDS_AS u32x2*)(uintptr_t)lds_off; }
__device__ __forceinline__ bf16x8 cat(s16x4 a, s16x4 b) { return __builtin_shufflevector(a, b, 0, 1, 2, 3, 4, 5, 6, 7); }
template <int N> __device__ __forceinline__ void wait_vm() { asm volatile("s_waitcnt vmcnt(%0)" ::"n"(N) : "memory"); }
__device__ __forceinline__ void wait_lgkm0() { asm volatile("s_waitcnt lgkmcnt(0)" ::: "memory"); }
__device__ __forceinline__ void bar() { __builtin_amdgcn_s_barrier(); }
#define MFMA(a, b, c) __builtin_amdgcn_mfma_f32_16x16x32_bf16(a, b, c, 0, 0, 0)

__device__ __forceinline__ void swap16(float v, float& a, float& b) {
  a = v; b = v;
  asm volatile("s_nop 1\n\tv_permlane16_swap_b32 %0, %1" : "+v"(a), "+v"(b));
}
__device__ __forceinline__ void swap32(float v, float& a, float& b) {
  a = v; b = v;
  asm volatile("s_nop 1\n\tv_permlane32_swap_b32 %0, %1" : "+v"(a), "+v"(b));
}
__device__ __forceinline__ float xsum4(float v) {
  float a, b;
  swap16(v, a, b); v = a + b;
  swap32(v, a, b); return a + b;
}
__device__ __forceinline__ float xmax4(float v) {
  float a, b;
  swap16(v, a, b); v = fmaxf(a, b);
  swap32(v, a, b); return fmaxf(a, b);
}
__device__ __forceinline__ float swap1(float v) {
  return v + __builtin_bit_cast(float, __builtin_amdgcn_update_dpp(0, __builtin_bit_cast(int, v), 0xB1, 0xF, 0xF, true));
}
#define MFMA32(a, b, c) __builtin_amdgcn_mfma_f32_32x32x16_bf16(a, b, c, 0, 0, 0)
#define SB() __builtin_amdgcn_sched_barrier(0)
#define RDW(base, e) cat(tr_read((base) + (e) * 8192), tr_read((base) + (e) * 8192 + 1024))
#define STAT(d) do {                                                                                     \
    if ((d) == 0 || (d) == 4 || (d) == 9) { SB(); sq[(d) >> 2] = MFMA(sv[(d) >> 2], sv[(d) >> 2], sq[(d) >> 2]); }   \
    if ((d) == 2 || (d) == 7 || (d) == 11) { SB(); sx[(d) >> 2] = MFMA(sel, sv[(d) >> 2], sx[(d) >> 2]); }           \
  } while (0)
#define CSUM(e) do { SB(); cs[e] = MFMA(wa[e], sel, cs[e]); } while (0)
#define K1_STEP(EVEN, MORE)                                                                              \
  do {                                                                                                   \
                 \
    const unsigned xh = xb ^ 32u;                                                                        \
    acc[0][0] = MFMA32(wa[0], xa[0], acc[0][0]); wc[0] = RDW(wb1, 0); xc[0] = lds_read16(xh); SB();       \
    acc[1][0] = MFMA32(wa[0], xa[1], acc[1][0]); xc[1] = lds_read16(xh + 4096); xc[2] = lds_read16(xh + 8192); \
    if (EVEN) sv[0] = lds_read16(sb_); SB();                                   \
    acc[2][0] = MFMA32(wa[0], xa[2], acc[2][0]); wc[1] = RDW(wb1, 1); CSUM(0); SB();                   \
    acc[0][1] = MFMA32(wa[1], xa[0], acc[0][1]);                                              \
    if (EVEN) { sv[1] = lds_read16(sb_ + 1024); sv[2] = lds_read16(sb_ + 2048); } \
    else STAT(6);                                                                                        \
    SB();                                                                                                \
    acc[1][1] = MFMA32(wa[1], xa[1], acc[1][1]); if (MORE) glds16(ws0, wo0, wdst, wfill); SB(); \
    acc[2][1] = MFMA32(wa[1], xa[2], acc[2][1]); if (MORE) glds16(ws1, wo1, wdst + 8192, wfill); CSUM(1); \
    wb0 += winc; wb1 += winc; if (EVEN) xb ^= 64u; else { xb = (xb ^ 64u) + xinc; sb_ += xinc; } SB();    \
                      \
    acc[0][0] = MFMA32(wc[0], xc[0], acc[0][0]); wa[0] = RDW(wb0, 0); xa[0] = lds_read16(xb); SB();       \
    acc[1][0] = MFMA32(wc[0], xc[1], acc[1][0]); xa[1] = lds_read16(xb + 4096); xa[2] = lds_read16(xb + 8192); \
    STAT(EVEN ? 0 : 7); SB();                                                                            \
    acc[2][0] = MFMA32(wc[0], xc[2], acc[2][0]); wa[1] = RDW(wb0, 1); STAT(EVEN ? 1 : 8); SB();           \
    acc[0][1] = MFMA32(wc[1], xc[0], acc[0][1]); STAT(EVEN ? 2 : 9);                                      \
    if (EVEN && MORE) glds16(xs, xo[0], xdst, xfill); SB();                                               \
    acc[1][1] = MFMA32(wc[1], xc[1], acc[1][1]); STAT(EVEN ? 3 : 10);                                     \
    if (EVEN && MORE) glds16(xs, xo[1], xdst + 1024, xfill); SB();                                        \
    acc[2][1] = MFMA32(wc[1], xc[2], acc[2][1]); STAT(EVEN ? 4 : 11);                                     \
    if (EVEN && MORE) glds16(xs, xo[2], xdst + 2048, xfill);                                              \
    if (EVEN) STAT(5);                                                                                   \
    SB();                                                                                                \
  } while (0)
#define K1_PAIR(MORE, W0, W1)                                                                            \
  do {                                                                                                   \
    int winc = wfill == 3 * WSLOT ? -4 * WSLOT : WSLOT;                                                  \
    const int xinc = xfill == XSLOT ? -2 * XSLOT : XSLOT;                                                \
    K1_STEP(true, MORE);                                                                                 \
    ws0 += wstep; ws1 += wstep; xs += 128;                                                               \
    wfill = wfill == 4 * WSLOT ? 0 : wfill + WSLOT;                                                      \
    xfill = xfill == 2 * XSLOT ? 0 : xfill + XSLOT;                                                      \
    winc = wfill == 3 * WSLOT ? -4 * WSLOT : WSLOT;                                                      \
    wait_vm<W0>();                                                                                       \
    bar();                                                                                               \
    K1_STEP(false, MORE);                                                                                \
    ws0 += wstep; ws1 += wstep;                                                                          \
    wfill = wfill == 4 * WSLOT ? 0 : wfill + WSLOT;                                                      \
    wait_vm<W1>();                                                                                       \
    bar();                                                                                               \
  } while (0)

__global__ __launch_bounds__(512, 2) void k1_kernel(const char* __restrict__ x, const char* __restrict__ wqkv,
                                                     const char* __restrict__ wo, const char* __restrict__ wg,
                                                     const char* __restrict__ wu, const char* __restrict__ wd,
                                                     char* __restrict__ act, char* __restrict__ w2p) {
  extern __shared__ __attribute__((aligned(16))) char smem[];
  const int tid = threadIdx.x, l = tid & 63;
  const int w = __builtin_amdgcn_readfirstlane(tid >> 6);
  const int wn = w & 3, wm = w >> 2;
  const int b = blockIdx.x, xcd = b & 7, idx = b >> 3;
  const bool isA = xcd < 3;
  const int lin = xcd * 32 + idx, f0 = lin - 96;
  const int fh = f0 >= 84, f1 = fh ? f0 - 84 : f0;
  const int fct = fh ? f1 / 5 : f1 / 6;
  const int ct = isA ? (lin & 7) : fct;
  const int rt = isA ? (lin >> 3) : (fh ? 6 + f1 - fct * 5 : f1 - fct * 6);
  const int ct_ = ct, rt_ = rt;
  STAMP(act, 0); STAMPR(act, 8);
  const int g4 = l >> 4, l15 = l & 15;
  const unsigned sbase = (unsigned)(uintptr_t)(LDS_AS char*)smem;

  const int psb = f0 * 5 + w;
  const bool dopack = !isA && w < 5 && psb < 740;
  const int pks = psb / 10, pnb = psb - pks * 10;
  u32x2 pv[8];
#define PACK_LOAD()                                                                                                     \
  do {                                                                                                                  \
    const char* p_ = (pks < 20 ? wo + (long)(32 * pks) * 1280 : wd + (long)(32 * (pks - 20)) * 1280) + 128 * pnb;       \
    const unsigned ro = (pks < 20 ? 8 * g4 : 16 * (g4 >> 1) + 4 * (g4 & 1)) * 1280 + 8 * l15;                           \
    const unsigned r4 = ro + (pks < 20 ? 4 : 8) * 1280;                                                                 \
    _Pragma("unroll") for (int j = 0; j < 8; ++j) pv[j] = *(const u32x2*)(p_ + ((j < 4 ? ro : r4) + (j & 3) * 1280));   \
  } while (0)

  if (f0 < 154) {
    const int t0 = isA ? 176 * rt - 16 : 192 * rt;
    unsigned xo[3];
#pragma unroll
    for (int i = 0; i < 3; ++i) {
      const int row = 24 * w + 8 * i + (l >> 3);
      int gr = t0 + row;
      gr = gr < 0 ? 0 : (gr > S_ - 1 ? S_ - 1 : gr);
      xo[i] = gr * 1280 + (((l & 7) ^ ((row >> 1) & 7)) << 4);
    }
    const unsigned xdst = sbase + X_OFF + w * 3072, wdst = sbase + W_OFF + w * 1024;
    const char* xs = x;
#define ISSUE_X(slot_off) do { glds16(xs, xo[0], xdst, slot_off); glds16(xs, xo[1], xdst + 1024, slot_off); \
                               glds16(xs, xo[2], xdst + 2048, slot_off); xs += 128; } while (0)
    ISSUE_X(0);
    const int wstride = isA ? 3840 : 3456;
    unsigned wo0, wo1;
    {
      const int c16 = l15 ^ (g4 << 2);
      const int rowo = (4 * w + g4) * wstride;
      const int a0 = c16 * 16 + (c16 < 10 ? 1280 : 2400) + ct * 160;
      const int a1 = (c16 < 14 ? c16 : c16 - 2) * 16 + (c16 < 4 ? 2656 : -64) + ct * 160;
      const int fc = ct * 256 + c16 * 16;
      const int f = fc >= 2 * F_ ? fc - 128 : fc;
      wo0 = rowo + (isA ? a0 : f);
      wo1 = rowo + (isA ? a1 : f);
    }
    const char *ws0 = isA ? wqkv : wg, *ws1 = isA ? wqkv : wu;
    const int wstep = 32 * wstride;
#define ISSUE_W(slot_off) do { glds16(ws0, wo0, wdst, slot_off); glds16(ws1, wo1, wdst + 8192, slot_off); \
                               ws0 += wstep; ws1 += wstep; } while (0)
    ISSUE_W(0); ISSUE_W(WSLOT);
    const int hf = l >> 5, l31 = l & 31;
    unsigned xb = sbase + X_OFF + (96 * wm + l31) * 128 + 16 * (unsigned)((2 * wm + hf) ^ ((l31 >> 1) & 7));
    unsigned wb0, wb1;
    {
      const int q = l15 >> 2;
      const unsigned wbase = sbase + W_OFF + (8 * hf + q) * 256 + 64 * (wn ^ q) + 32 * (g4 & 1) + 8 * (l & 3);
      wb0 = wbase + wm * 4096;
      wb1 = wbase + (1 - wm) * 4096;
    }
    f32x16 acc[3][2];
    f32x4 sx[3], sq[3];
    f32x4 cs[2];
#pragma unroll
    for (int i = 0; i < 3; ++i) {
      sx[i] = sq[i] = f32x4{0.f, 0.f, 0.f, 0.f};
#pragma unroll
      for (int r = 0; r < 16; ++r) { acc[i][0][r] = 0.f; acc[i][1][r] = 0.f; }
    }
    cs[0] = cs[1] = f32x4{0.f, 0.f, 0.f, 0.f};
    const unsigned s1_ = l15 == (g4 & 1) ? 0x3f803f80u : 0u;
    u32x4 sel_ = {s1_, s1_, s1_, s1_};
    asm volatile("" : "+v"(sel_));
    const bf16x8 sel = __builtin_bit_cast(bf16x8, sel_);
    unsigned sb_ = xdst + (l15 >> 1) * 128 + 16 * ((2 * g4 + (l15 & 1)) ^ (2 * (l15 >> 2)));
    ISSUE_X(XSLOT); ISSUE_W(2 * WSLOT);
    {
      bf16x8 xa[3], xc[3], wa[2], wc[2];
      bf16x8 sv[3];
      wait_vm<7>();
      bar();
      STAMP(act, 1);
      ISSUE_W(3 * WSLOT);
#pragma unroll
      for (int i = 0; i < 3; ++i) xa[i] = lds_read16(xb + i * 4096);
      wa[0] = RDW(wb0, 0); wa[1] = RDW(wb0, 1);
      wait_vm<7>();
      bar();
      int wfill = 4 * WSLOT, xfill = 2 * XSLOT;
#pragma nounroll
      for (int s = 0; s < 16; s += 2) K1_PAIR(true, 7, 7);
#pragma nounroll
      for (int s = 16; s < NS; s += 2) K1_PAIR(false, 2, 0);
      wait_lgkm0();
      bar();
      STAMP(act, 2);
    }
    {
      int l = tid & 63, wn = w & 3, wm = w >> 2, ct = ct_, rt = rt_;
      asm volatile("" : "+v"(l), "+s"(wm), "+s"(wn), "+s"(ct), "+s"(rt));
      const int g4 = l >> 4, l15 = l & 15, hf = l >> 5, l31 = l & 31;
      if (dopack) PACK_LOAD();
      float* csp = (float*)(smem + CS_OFF);
#pragma unroll
      for (int i = 0; i < 3; ++i) {
        const float a = swap1(sx[i][0] + sx[i][1]);
        const float q = swap1((l15 & 2) ? ((l15 & 1) ? sq[i][3] : sq[i][2]) : ((l15 & 1) ? sq[i][1] : sq[i][0]));
        float* sp = csp + 1024 + 2 * (24 * w + 8 * i + (l15 >> 1));
        if (!(l15 & 1) && g4 == 0) sp[0] = a;
        if (!(l15 & 1) && g4 == (l15 >> 2)) sp[1] = q;
      }
      if (l15 < 2) {
        *(f32x4*)(csp + wm * 256 + 32 * wn + 16 * l15 + 4 * g4) = cs[0];
        *(f32x4*)(csp + wm * 256 + 128 + 32 * wn + 16 * l15 + 4 * g4) = cs[1];
      }
      __syncthreads();
      float mean[3], rstd[3], vare[3];
#pragma unroll
      for (int i = 0; i < 3; ++i) {
        const f32x2 mv = *(const f32x2*)(csp + 1024 + 2 * (96 * wm + 32 * i + l31));
        mean[i] = mv[0] * (1.0f / D_);
        vare[i] = fmaxf(mv[1] * (1.0f / D_) - mean[i] * mean[i], 0.f) + 1e-5f;
        rstd[i] = __builtin_amdgcn_rsqf(vare[i]);
      }
      const float* cq = csp + 32 * wn + 4 * hf;
#define COLSUM(e, rq) (*(const f32x4*)(cq + 128 * (e) + 8 * (rq)) + *(const f32x4*)(cq + 256 + 128 * (e) + 8 * (rq)))
#define SUB4(v, rq) f32x4{(v)[4 * (rq)], (v)[4 * (rq) + 1], (v)[4 * (rq) + 2], (v)[4 * (rq) + 3]}
      if (!isA) {
        const bool colok = ct * 128 + wn * 32 < F_;
        const float c1 = -2.0f * 0.7978845608028654f * 1.4426950408889634f, c3 = c1 * 0.044715f;
        const int ks = 20 + 4 * ct + wn;
#pragma unroll
        for (int half = 0; half < 2; ++half) {
          f32x4 cg[2], cu[2];
#pragma unroll
          for (int a = 0; a < 2; ++a) { cg[a] = COLSUM(0, 2 * half + a); cu[a] = COLSUM(1, 2 * half + a); }
#pragma unroll
          for (int i = 0; i < 3; ++i) {
            const float k1 = c1 * rstd[i], k3 = c3 * rstd[i] * rstd[i] * rstd[i];
            unsigned pk[4];
#pragma unroll
            for (int a = 0; a < 2; ++a) {
              const f32x4 g = SUB4(acc[i][0], 2 * half + a) - mean[i] * cg[a];
              const f32x4 u = SUB4(acc[i][1], 2 * half + a) - mean[i] * cu[a];
              const f32x4 z = g * (g * g * k3 + k1);
              f32x4 e;
#pragma unroll
              for (int r = 0; r < 4; ++r) e[r] = __builtin_amdgcn_exp2f(z[r]);
              e = e * vare[i] + vare[i];
#pragma unroll
              for (int r = 0; r < 4; ++r) e[r] = __builtin_amdgcn_rcpf(e[r]);
              const f32x4 h = (g * u) * e;
              pk[2 * a] = pack2(h[0], h[1]); pk[2 * a + 1] = pack2(h[2], h[3]);
            }
            const int tu = 12 * rt + 6 * wm + 2 * i;
            if (colok && tu < 128)
              *(u32x4*)(act + (long)(tu * KT2 + ks) * 1024 + (l31 >> 4) * (KT2 * 1024) + (2 * half + hf) * 256 + l15 * 16) =
                  u32x4{pk[0], pk[1], pk[2], pk[3]};
          }
        }
      } else {
#pragma unroll
        for (int e = 0; e < 2; ++e)
#pragma unroll
          for (int rq = 0; rq < 4; ++rq) {
            const f32x4 c = COLSUM(e, rq);
            if (e == 0 || wn < 3 || rq < 2) {
#pragma unroll
              for (int i = 0; i < 3; ++i) {
                const f32x4 v = (SUB4(acc[i][e], rq) - mean[i] * c) * rstd[i];
                *(u32x2*)(smem + (96 * wm + 32 * i + l31) * QROW + (128 * e + 32 * wn + 8 * rq + 4 * hf) * 2) =
                    u32x2{pack2(v[0], v[1]), pack2(v[2], v[3])};
              }
            }
          }
        __syncthreads();
        const float sc = 0.22360679774997896f * 1.4426950408889634f;
        const bool lo = g4 < 3, hi = g4 < 2;
        const int dq = l15 - 4 * g4;
        const bool par = l15 & 1;
        const int dA = dq - (l15 & 1), dB = dA - 2;
        const bool curA = dA >= 0, curB = dB >= 0;
        const unsigned psh = par ? 16u : 0u;
        const int nit = rt == 11 ? 14 : 22;
#pragma nounroll
        for (int it = w; it < nit; it += 8) {
          const int iu = 1 + (it >> 1);
          const int tu = 11 * rt - 1 + iu;
          const float bA = (curA || tu > 0) ? 0.f : -3.0e38f, bB = (curB || tu > 0) ? 0.f : -3.0e38f;
          const f32x4 z4 = {0.f, 0.f, 0.f, 0.f};
          u32x2 oa[2], ob[2];
#pragma unroll
          for (int e = 0; e < 2; ++e) {
            const int hh = 2 * (it & 1) + e;
            const unsigned rq = sbase + (16 * iu + l15) * QROW + 40 * hh + 16 * g4;
            const u32x2 q0 = lds_read8(rq + 320), q1 = lds_read8(rq + 328);
            const u32x2 a0 = lds_read8(rq - 16 * QROW), a1 = lds_read8(rq - 16 * QROW + 8);
            const u32x2 b0 = lds_read8(rq), b1 = lds_read8(rq + 8);
            const unsigned rv = sbase + (16 * iu + (l >> 2)) * QROW + 160 + 40 * hh + 8 * (l & 3);
            const s16x4 v00 = tr_read(rv - 16 * QROW), v01 = tr_read(rv);
            const s16x4 v10 = tr_read(rv - 16 * QROW + 32), v11 = tr_read(rv + 32);
            const u32x4 qf = {lo ? q0[0] : 0u, lo ? q0[1] : 0u, hi ? q1[0] : 0u, hi ? q1[1] : 0u};
            const u32x4 kp = {a0[0], a0[1], a1[0], a1[1]};
            const u32x4 kc = {b0[0], b0[1], b1[0], b1[1]};
            const f32x4 s0 = MFMA(__builtin_bit_cast(bf16x8, kp), __builtin_bit_cast(bf16x8, qf), z4);
            const f32x4 s1 = MFMA(__builtin_bit_cast(bf16x8, kc), __builtin_bit_cast(bf16x8, qf), z4);
            const float sA = __builtin_fmaf(curA ? (par ? s1[1] : s1[0]) : (par ? s0[1] : s0[0]), sc, bA);
            const float sB = __builtin_fmaf(curB ? (par ? s1[3] : s1[2]) : (par ? s0[3] : s0[2]), sc, bB);
            const float m = xmax4(fmaxf(sA, sB));
            const float eA = __builtin_amdgcn_exp2f(sA - m), eB = __builtin_amdgcn_exp2f(sB - m);
            const float inv = __builtin_amdgcn_rcpf(xsum4(eA + eB));
            const unsigned pk = pack2(eA * inv, eB * inv);
            const unsigned pA = (pk & 0xffffu) << psh, pB = (pk >> 16) << psh;
            const u32x4 pf = {curA ? 0u : pA, curB ? 0u : pB, curA ? pA : 0u, curB ? pB : 0u};
            const f32x4 o0 = MFMA(cat(v00, v01), __builtin_bit_cast(bf16x8, pf), z4);
            const f32x4 o1 = MFMA(cat(v10, v11), __builtin_bit_cast(bf16x8, pf), z4);
            oa[e] = u32x2{pack2(o0[0], o0[1]), pack2(o0[2], o0[3])};
            ob[e] = u32x2{pack2(o1[0], o1[1]), pack2(o1[2], o1[3])};
          }
          {
            char* ab = act + (long)tu * (KT2 * 1024) + l15 * 16;
#pragma unroll
            for (int e = 0; e < 2; ++e) {
              const int c0 = 80 * ct + 40 * (it & 1) + 20 * e;
              const int col = c0 + 4 * g4, col2 = c0 + 16;
              *(u32x2*)(ab + (col >> 3) * 256 + (col & 4) * 2) = oa[e];
              if (g4 == 0) *(u32x2*)(ab + (col2 >> 3) * 256 + (col2 & 4) * 2) = ob[e];
            }
          }
        }
      }
    }
  }

  STAMP(act, 3);
  if (dopack) {
    if (f0 >= 154) PACK_LOAD();
    char* dst = w2p + (long)((4 * pnb + (l15 >> 2)) * KT2 + pks) * 1024 + g4 * 256 + (l15 & 3) * 64;
#pragma unroll
    for (int i = 0; i < 4; ++i) {
      const unsigned sel = (i & 1) ? 0x07060302u : 0x05040100u;
      u32x4 o;
#pragma unroll
      for (int jj = 0; jj < 4; ++jj) o[jj] = __builtin_amdgcn_perm(pv[2 * jj + 1][i >> 1], pv[2 * jj][i >> 1], sel);
      *(u32x4*)(dst + i * 16) = o;
    }
  }
  STAMP(act, 4); STAMPR(act, 9);
}

constexpr int K2D = 6;
__global__ __launch_bounds__(256, 1) void k2_kernel(const char* __restrict__ x, const char* __restrict__ act,
                                                     const char* __restrict__ w2p, char* __restrict__ out) {
  extern __shared__ __attribute__((aligned(16))) char smem[];
  const int tid = threadIdx.x, l = tid & 63;
  const int w = __builtin_amdgcn_readfirstlane(tid >> 6);
  const int b = blockIdx.x, xcd = b & 7, idx = b >> 3;
  const int rt = (xcd >> 1) * 8 + (idx >> 2), ct = (xcd & 1) * 4 + (idx & 3);
  const char* ap = act + (long)(4 * rt * KT2 + w) * 1024;
  const char* bp = w2p + (long)(5 * ct * KT2 + w) * 1024;
  const unsigned lo = l * 16;
  unsigned la[4];
#pragma unroll
  for (int i = 0; i < 4; ++i) la[i] = lo + (i ^ w) * (KT2 * 1024);
  STAMP(act + (1 << 20), 0); STAMPR(act + (1 << 20), 8);
  bf16x8 fa[K2D][4], fb[K2D][5];
  f32x4 acc[4][5];
#define K2_LOAD(d)                                                                                    \
  do {                                                                                                \
    _Pragma("unroll") for (int u = 0; u < 5; ++u) fb[d][u] = *(const bf16x8*)(bp + (lo + u * (KT2 * 1024))); \
    _Pragma("unroll") for (int i = 0; i < 4; ++i) fa[d][i] = *(const bf16x8*)(ap + la[i]); \
    ap += 4096; bp += 4096;                                                                           \
  } while (0)
#define K2_MMA(d)                                                                                     \
  do {                                                                                                \
    _Pragma("unroll") for (int u = 0; u < 5; ++u)                                                     \
    _Pragma("unroll") for (int i = 0; i < 4; ++i)                                                     \
      acc[i][u] = __builtin_amdgcn_mfma_f32_16x16x32_bf16(fb[d][u], fa[d][i], acc[i][u], 0, 0, 0);    \
  } while (0)
#pragma unroll
  for (int d = 0; d < K2D; ++d) K2_LOAD(d);
#pragma unroll
  for (int i = 0; i < 4; ++i)
#pragma unroll
    for (int u = 0; u < 5; ++u) acc[i][u] = f32x4{0.f, 0.f, 0.f, 0.f};
  const int g4 = l >> 4, l15 = l & 15;
  const unsigned ooff = (16 * (4 * rt + w) + l15) * 1280 + (80 * ct + 4 * g4) * 2;
  __builtin_amdgcn_sched_barrier(0);
  STAMP(act + (1 << 20), 1);
#pragma nounroll
  for (int r = 0; r < 18 / K2D - 1; ++r) {
#pragma unroll
    for (int d = 0; d < K2D; ++d) {
      K2_MMA(d);
      K2_LOAD(d);
      __builtin_amdgcn_sched_barrier(0);
    }
  }
  STAMP(act + (1 << 20), 2);
  K2_MMA(0);
  if (w < 2) K2_LOAD(0);
  u32x2 xr[5];
#pragma unroll
  for (int u = 0; u < 5; ++u) xr[u] = *(const u32x2*)(x + (ooff + u * 32));
  __builtin_amdgcn_sched_barrier(0);
#pragma unroll
  for (int d = 1; d < K2D; ++d) K2_MMA(d);
  if (w < 2) K2_MMA(0);
  STAMP(act + (1 << 20), 3);
  f32x4* red = (f32x4*)smem;
#pragma unroll
  for (int i = 1; i < 4; ++i)
#pragma unroll
    for (int u = 0; u < 5; ++u) red[(((i ^ w) * 3 + i - 1) * 5 + u) * 64 + l] = acc[i][u];
  __syncthreads();
#pragma unroll
  for (int u = 0; u < 5; ++u) {
    const f32x4 s = (acc[0][u] + red[((w * 3 + 0) * 5 + u) * 64 + l]) +
                    (red[((w * 3 + 1) * 5 + u) * 64 + l] + red[((w * 3 + 2) * 5 + u) * 64 + l]);
    const float x0 = __builtin_bit_cast(float, xr[u][0] << 16), x1 = __builtin_bit_cast(float, xr[u][0] & 0xffff0000u);
    const float x2 = __builtin_bit_cast(float, xr[u][1] << 16), x3 = __builtin_bit_cast(float, xr[u][1] & 0xffff0000u);
    u32x2 o = {pack2(x0 + s[0], x1 + s[1]), pack2(x2 + s[2], x3 + s[3])};
    *(u32x2*)(out + (ooff + u * 32)) = o;
  }
  STAMP(act + (1 << 20), 4); STAMPR(act + (1 << 20), 9);
}

extern "C" void kernel_launch(void* const* d_in, const int* in_sizes, int n_in, void* d_out, int out_size,
                              void* d_ws, size_t ws_size, hipStream_t stream) {
  const char* x = (const char*)d_in[0];
  const char* wqkv = (const char*)d_in[1];
  const char* wo = (const char*)d_in[2];
  const char* wg = (const char*)d_in[4];
  const char* wu = (const char*)d_in[5];
  const char* wd = (const char*)d_in[6];
  char* act = (char*)d_ws;
  char* w2p = act + (size_t)128 * KT2 * 1024;
  (void)hipFuncSetAttribute((const void*)k1_kernel, hipFuncAttributeMaxDynamicSharedMemorySize, LDS1);
  (void)hipFuncSetAttribute((const void*)k2_kernel, hipFuncAttributeMaxDynamicSharedMemorySize, 61440);
  k1_kernel<<<256, 512, LDS1, stream>>>(x, wqkv, wo, wg, wu, wd, act, w2p);
  k2_kernel<<<256, 256, 61440, stream>>>(x, act, w2p, (char*)d_out);
}
```

```cpp
#include <hip/hip_runtime.h>
#include <hip/hip_bf16.h>
#include <stdint.h>

typedef __attribute__((ext_vector_type(8))) short bf16x8;
typedef __attribute__((ext_vector_type(4))) short s16x4;
typedef __attribute__((ext_vector_type(4))) float f32x4;
typedef __attribute__((ext_vector_type(2))) float f32x2;
typedef __attribute__((ext_vector_type(16))) float f32x16;
typedef __attribute__((ext_vector_type(2))) __bf16 bf2;
typedef __attribute__((ext_vector_type(4))) unsigned u32x4;
typedef __attribute__((ext_vector_type(2))) unsigned u32x2;
#define LDS_AS __attribute__((address_space(3)))
#define STAMP(base, i)
#define STAMPR(base, i)

constexpr int S_ = 2048, D_ = 640, F_ = 1728;
constexpr int KT2 = 74;
constexpr int NS = 20;
constexpr int WSLOT = 16384, XSLOT = 24576;
constexpr int W_OFF = 0, X_OFF = 5 * WSLOT;
constexpr int LDS1 = 5 * WSLOT + 3 * XSLOT;
constexpr int QROW = 488;
constexpr int CS_OFF = 192 * QROW;

__device__ __forceinline__ float dot2(unsigned a, unsigned b, float c) {
  return __builtin_amdgcn_fdot2_f32_bf16(__builtin_bit_cast(bf2, a), __builtin_bit_cast(bf2, b), c, false);
}
__device__ __forceinline__ unsigned pack2(float a, float b) {
  f32x2 p = {a, b};
  return __builtin_bit_cast(unsigned, __builtin_convertvector(p, bf2));
}
__device__ __forceinline__ void glds16(const char* base, unsigned voff, unsigned lds_dst, unsigned lds_off) {
  asm volatile("s_add_u32 m0, %2, %3\n\ts_nop 0\n\tglobal_load_lds_dwordx4 %0, %1" ::"v"(voff), "s"(base), "s"(lds_dst), "s"(lds_off)
               : "memory", "scc");
}
__device__ __forceinline__ s16x4 tr_read(unsigned lds_off) {
  return __builtin_amdgcn_ds_read_tr16_b64_v4i16((LDS_AS s16x4*)(uintptr_t)lds_off);
}
__device__ __forceinline__ bf16x8 lds_read16(unsigned lds_off) { return *(LDS_AS bf16x8*)(uintptr_t)lds_off; }
__device__ __forceinline__ u32x2 lds_read8(unsigned lds_off) { return *(LDS_AS u32x2*)(uintptr_t)lds_off; }
__device__ __forceinline__ bf16x8 cat(s16x4 a, s16x4 b) { return __builtin_shufflevector(a, b, 0, 1, 2, 3, 4, 5, 6, 7); }
template <int N> __device__ __forceinline__ void wait_vm() { asm volatile("s_waitcnt vmcnt(%0)" ::"n"(N) : "memory"); }
__device__ __forceinline__ void wait_lgkm0() { asm volatile("s_waitcnt lgkmcnt(0)" ::: "memory"); }
__device__ __forceinline__ void bar() { __builtin_amdgcn_s_barrier(); }
#define MFMA(a, b, c) __builtin_amdgcn_mfma_f32_16x16x32_bf16(a, b, c, 0, 0, 0)

__device__ __forceinline__ void swap16(float v, float& a, float& b) {
  a = v; b = v;
  asm volatile("s_nop 1\n\tv_permlane16_swap_b32 %0, %1" : "+v"(a), "+v"(b));
}
__device__ __forceinline__ void swap32(float v, float& a, float& b) {
  a = v; b = v;
  asm volatile("s_nop 1\n\tv_permlane32_swap_b32 %0, %1" : "+v"(a), "+v"(b));
}
__device__ __forceinline__ float xsum4(float v) {
  float a, b;
  swap16(v, a, b); v = a + b;
  swap32(v, a, b); return a + b;
}
__device__ __forceinline__ float xmax4(float v) {
  float a, b;
  swap16(v, a, b); v = fmaxf(a, b);
  swap32(v, a, b); return fmaxf(a, b);
}
__device__ __forceinline__ float swap1(float v) {
  return v + __builtin_bit_cast(float, __builtin_amdgcn_update_dpp(0, __builtin_bit_cast(int, v), 0xB1, 0xF, 0xF, true));
}
#define MFMA32(a, b, c) __builtin_amdgcn_mfma_f32_32x32x16_bf16(a, b, c, 0, 0, 0)
#define SB() __builtin_amdgcn_sched_barrier(0)
#define RDW(base, e) cat(tr_read((base) + (e) * 8192), tr_read((base) + (e) * 8192 + 1024))
#define ROWSUM(i) do { sq[i] = MFMA(sv[i], sv[i], sq[i]); sx[i] = MFMA(sel, sv[i], sx[i]); } while (0)
#define CSUM(e) do { SB(); cs[e] = MFMA(wa[e], sel, cs[e]); } while (0)
#define K1_STEP(EVEN, MORE)                                                                              \
  do {                                                                                                   \
                 \
    const unsigned xh = xb ^ 32u;                                                                        \
    acc[0][0] = MFMA32(wa[0], xa[0], acc[0][0]); wc[0] = RDW(wb1, 0); xc[0] = lds_read16(xh); SB();       \
    acc[1][0] = MFMA32(wa[0], xa[1], acc[1][0]); xc[1] = lds_read16(xh + 4096); xc[2] = lds_read16(xh + 8192); \
    if (EVEN) sv[0] = lds_read16(sb_); SB();                                   \
    acc[2][0] = MFMA32(wa[0], xa[2], acc[2][0]); wc[1] = RDW(wb1, 1); SB();                   \
    acc[0][1] = MFMA32(wa[1], xa[0], acc[0][1]);                                              \
    if (EVEN) { sv[1] = lds_read16(sb_ + 1024); sv[2] = lds_read16(sb_ + 2048); } \
    SB();                                                                                                \
    acc[1][1] = MFMA32(wa[1], xa[1], acc[1][1]); if (MORE) glds16(ws0, wo0, wdst, wfill); SB(); \
    acc[2][1] = MFMA32(wa[1], xa[2], acc[2][1]); if (MORE) glds16(ws1, wo1, wdst + 8192, wfill); CSUM(0); CSUM(1); if (!(EVEN)) { ROWSUM(0); ROWSUM(1); ROWSUM(2); } \
    wb0 += winc; wb1 += winc; if (EVEN) xb ^= 64u; else { xb = (xb ^ 64u) + xinc; sb_ += xinc; } SB();    \
                      \
    acc[0][0] = MFMA32(wc[0], xc[0], acc[0][0]); wa[0] = RDW(wb0, 0); xa[0] = lds_read16(xb); SB();       \
    acc[1][0] = MFMA32(wc[0], xc[1], acc[1][0]); xa[1] = lds_read16(xb + 4096); xa[2] = lds_read16(xb + 8192); \
    SB();                                                                            \
    acc[2][0] = MFMA32(wc[0], xc[2], acc[2][0]); wa[1] = RDW(wb0, 1); SB();           \
    acc[0][1] = MFMA32(wc[1], xc[0], acc[0][1]);                                      \
    if (EVEN && MORE) glds16(xs, xo[0], xdst, xfill); SB();                                               \
    acc[1][1] = MFMA32(wc[1], xc[1], acc[1][1]);                                     \
    if (EVEN && MORE) glds16(xs, xo[1], xdst + 1024, xfill); SB();                                        \
    acc[2][1] = MFMA32(wc[1], xc[2], acc[2][1]);                                     \
    if (EVEN && MORE) glds16(xs, xo[2], xdst + 2048, xfill);                                              \
    SB();                                                                                                \
  } while (0)
#define K1_PAIR(MORE, W0, W1)                                                                            \
  do {                                                                                                   \
    int winc = wfill == 3 * WSLOT ? -4 * WSLOT : WSLOT;                                                  \
    const int xinc = xfill == XSLOT ? -2 * XSLOT : XSLOT;                                                \
    K1_STEP(true, MORE);                                                                                 \
    ws0 += wstep; ws1 += wstep; xs += 128;                                                               \
    wfill = wfill == 4 * WSLOT ? 0 : wfill + WSLOT;                                                      \
    xfill = xfill == 2 * XSLOT ? 0 : xfill + XSLOT;                                                      \
    winc = wfill == 3 * WSLOT ? -4 * WSLOT : WSLOT;                                                      \
    wait_vm<W0>();                                                                                       \
    bar();                                                                                               \
    K1_STEP(false, MORE);                                                                                \
    ws0 += wstep; ws1 += wstep;                                                                          \
    wfill = wfill == 4 * WSLOT ? 0 : wfill + WSLOT;                                                      \
    wait_vm<W1>();                                                                                       \
    bar();                                                                                               \
  } while (0)

__global__ __launch_bounds__(512, 2) void k1_kernel(const char* __restrict__ x, const char* __restrict__ wqkv,
                                                     const char* __restrict__ wo, const char* __restrict__ wg,
                                                     const char* __restrict__ wu, const char* __restrict__ wd,
                                                     char* __restrict__ act, char* __restrict__ w2p) {
  extern __shared__ __attribute__((aligned(16))) char smem[];
  const int tid = threadIdx.x, l = tid & 63;
  const int w = __builtin_amdgcn_readfirstlane(tid >> 6);
  const int wn = w & 3, wm = w >> 2;
  const int b = blockIdx.x, xcd = b & 7, idx = b >> 3;
  const bool isA = xcd < 3;
  const int lin = xcd * 32 + idx, f0 = lin - 96;
  const int fh = f0 >= 84, f1 = fh ? f0 - 84 : f0;
  const int fct = fh ? f1 / 5 : f1 / 6;
  const int ct = isA ? (lin & 7) : fct;
  const int rt = isA ? (lin >> 3) : (fh ? 6 + f1 - fct * 5 : f1 - fct * 6);
  const int ct_ = ct, rt_ = rt;
  STAMP(act, 0); STAMPR(act, 8);
  const int g4 = l >> 4, l15 = l & 15;
  const unsigned sbase = (unsigned)(uintptr_t)(LDS_AS char*)smem;

  const int psb = f0 * 5 + w;
  const bool dopack = !isA && w < 5 && psb < 740;
  const int pks = psb / 10, pnb = psb - pks * 10;
  u32x2 pv[8];
#define PACK_LOAD()                                                                                                     \
  do {                                                                                                                  \
    const char* p_ = (pks < 20 ? wo + (long)(32 * pks) * 1280 : wd + (long)(32 * (pks - 20)) * 1280) + 128 * pnb;       \
    const unsigned ro = (pks < 20 ? 8 * g4 : 16 * (g4 >> 1) + 4 * (g4 & 1)) * 1280 + 8 * l15;                           \
    const unsigned r4 = ro + (pks < 20 ? 4 : 8) * 1280;                                                                 \
    _Pragma("unroll") for (int j = 0; j < 8; ++j) pv[j] = *(const u32x2*)(p_ + ((j < 4 ? ro : r4) + (j & 3) * 1280));   \
  } while (0)

  if (f0 < 154) {
    const int t0 = isA ? 176 * rt - 16 : 192 * rt;
    unsigned xo[3];
#pragma unroll
    for (int i = 0; i < 3; ++i) {
      const int row = 24 * w + 8 * i + (l >> 3);
      int gr = t0 + row;
      gr = gr < 0 ? 0 : (gr > S_ - 1 ? S_ - 1 : gr);
      xo[i] = gr * 1280 + (((l & 7) ^ ((row >> 1) & 7)) << 4);
    }
    const unsigned xdst = sbase + X_OFF + w * 3072, wdst = sbase + W_OFF + w * 1024;
    const char* xs = x;
#define ISSUE_X(slot_off) do { glds16(xs, xo[0], xdst, slot_off); glds16(xs, xo[1], xdst + 1024, slot_off); \
                               glds16(xs, xo[2], xdst + 2048, slot_off); xs += 128; } while (0)
    ISSUE_X(0);
    const int wstride = isA ? 3840 : 3456;
    unsigned wo0, wo1;
    {
      const int c16 = l15 ^ (g4 << 2);
      const int rowo = (4 * w + g4) * wstride;
      const int a0 = c16 * 16 + (c16 < 10 ? 1280 : 2400) + ct * 160;
      const int a1 = (c16 < 14 ? c16 : c16 - 2) * 16 + (c16 < 4 ? 2656 : -64) + ct * 160;
      const int fc = ct * 256 + c16 * 16;
      const int f = fc >= 2 * F_ ? fc - 128 : fc;
      wo0 = rowo + (isA ? a0 : f);
      wo1 = rowo + (isA ? a1 : f);
    }
    const char *ws0 = isA ? wqkv : wg, *ws1 = isA ? wqkv : wu;
    const int wstep = 32 * wstride;
#define ISSUE_W(slot_off) do { glds16(ws0, wo0, wdst, slot_off); glds16(ws1, wo1, wdst + 8192, slot_off); \
                               ws0 += wstep; ws1 += wstep; } while (0)
    ISSUE_W(0); ISSUE_W(WSLOT);
    const int hf = l >> 5, l31 = l & 31;
    unsigned xb = sbase + X_OFF + (96 * wm + l31) * 128 + 16 * (unsigned)((2 * wm + hf) ^ ((l31 >> 1) & 7));
    unsigned wb0, wb1;
    {
      const int q = l15 >> 2;
      const unsigned wbase = sbase + W_OFF + (8 * hf + q) * 256 + 64 * (wn ^ q) + 32 * (g4 & 1) + 8 * (l & 3);
      wb0 = wbase + wm * 4096;
      wb1 = wbase + (1 - wm) * 4096;
    }
    f32x16 acc[3][2];
    f32x4 sx[3], sq[3];
    f32x4 cs[2];
#pragma unroll
    for (int i = 0; i < 3; ++i) {
      sx[i] = sq[i] = f32x4{0.f, 0.f, 0.f, 0.f};
#pragma unroll
      for (int r = 0; r < 16; ++r) { acc[i][0][r] = 0.f; acc[i][1][r] = 0.f; }
    }
    cs[0] = cs[1] = f32x4{0.f, 0.f, 0.f, 0.f};
    const unsigned s1_ = l15 == (g4 & 1) ? 0x3f803f80u : 0u;
    u32x4 sel_ = {s1_, s1_, s1_, s1_};
    asm volatile("" : "+v"(sel_));
    const bf16x8 sel = __builtin_bit_cast(bf16x8, sel_);
    unsigned sb_ = xdst + (l15 >> 1) * 128 + 16 * ((2 * g4 + (l15 & 1)) ^ (2 * (l15 >> 2)));
    ISSUE_X(XSLOT); ISSUE_W(2 * WSLOT);
    {
      bf16x8 xa[3], xc[3], wa[2], wc[2];
      bf16x8 sv[3];
      wait_vm<7>();
      bar();
      STAMP(act, 1);
      ISSUE_W(3 * WSLOT);
#pragma unroll
      for (int i = 0; i < 3; ++i) xa[i] = lds_read16(xb + i * 4096);
      wa[0] = RDW(wb0, 0); wa[1] = RDW(wb0, 1);
      wait_vm<7>();
      bar();
      int wfill = 4 * WSLOT, xfill = 2 * XSLOT;
#pragma nounroll
      for (int s = 0; s < 16; s += 2) K1_PAIR(true, 7, 7);
#pragma nounroll
      for (int s = 16; s < NS; s += 2) K1_PAIR(false, 2, 0);
      wait_lgkm0();
      bar();
      STAMP(act, 2);
    }
    {
      int l = tid & 63, wn = w & 3, wm = w >> 2, ct = ct_, rt = rt_;
      asm volatile("" : "+v"(l), "+s"(wm), "+s"(wn), "+s"(ct), "+s"(rt));
      const int g4 = l >> 4, l15 = l & 15, hf = l >> 5, l31 = l & 31;
      if (dopack) PACK_LOAD();
      float* csp = (float*)(smem + CS_OFF);
#pragma unroll
      for (int i = 0; i < 3; ++i) {
        const float a = swap1(sx[i][0] + sx[i][1]);
        const float q = swap1((l15 & 2) ? ((l15 & 1) ? sq[i][3] : sq[i][2]) : ((l15 & 1) ? sq[i][1] : sq[i][0]));
        float* sp = csp + 1024 + 2 * (24 * w + 8 * i + (l15 >> 1));
        if (!(l15 & 1) && g4 == 0) sp[0] = a;
        if (!(l15 & 1) && g4 == (l15 >> 2)) sp[1] = q;
      }
      if (l15 < 2) {
        *(f32x4*)(csp + wm * 256 + 32 * wn + 16 * l15 + 4 * g4) = cs[0];
        *(f32x4*)(csp + wm * 256 + 128 + 32 * wn + 16 * l15 + 4 * g4) = cs[1];
      }
      __syncthreads();
      float mean[3], rstd[3], vare[3];
#pragma unroll
      for (int i = 0; i < 3; ++i) {
        const f32x2 mv = *(const f32x2*)(csp + 1024 + 2 * (96 * wm + 32 * i + l31));
        mean[i] = mv[0] * (1.0f / D_);
        vare[i] = fmaxf(mv[1] * (1.0f / D_) - mean[i] * mean[i], 0.f) + 1e-5f;
        rstd[i] = __builtin_amdgcn_rsqf(vare[i]);
      }
      const float* cq = csp + 32 * wn + 4 * hf;
#define COLSUM(e, rq) (*(const f32x4*)(cq + 128 * (e) + 8 * (rq)) + *(const f32x4*)(cq + 256 + 128 * (e) + 8 * (rq)))
#define SUB4(v, rq) f32x4{(v)[4 * (rq)], (v)[4 * (rq) + 1], (v)[4 * (rq) + 2], (v)[4 * (rq) + 3]}
      if (!isA) {
        const bool colok = ct * 128 + wn * 32 < F_;
        const float c1 = -2.0f * 0.7978845608028654f * 1.4426950408889634f, c3 = c1 * 0.044715f;
        const int ks = 20 + 4 * ct + wn;
#pragma unroll
        for (int half = 0; half < 2; ++half) {
          f32x4 cg[2], cu[2];
#pragma unroll
          for (int a = 0; a < 2; ++a) { cg[a] = COLSUM(0, 2 * half + a); cu[a] = COLSUM(1, 2 * half + a); }
#pragma unroll
          for (int i = 0; i < 3; ++i) {
            const float k1 = c1 * rstd[i], k3 = c3 * rstd[i] * rstd[i] * rstd[i];
            unsigned pk[4];
#pragma unroll
            for (int a = 0; a < 2; ++a) {
              const f32x4 g = SUB4(acc[i][0], 2 * half + a) - mean[i] * cg[a];
              const f32x4 u = SUB4(acc[i][1], 2 * half + a) - mean[i] * cu[a];
              const f32x4 z = g * (g * g * k3 + k1);
              f32x4 e;
#pragma unroll
              for (int r = 0; r < 4; ++r) e[r] = __builtin_amdgcn_exp2f(z[r]);
              e = e * vare[i] + vare[i];
#pragma unroll
              for (int r = 0; r < 4; ++r) e[r] = __builtin_amdgcn_rcpf(e[r]);
              const f32x4 h = (g * u) * e;
              pk[2 * a] = pack2(h[0], h[1]); pk[2 * a + 1] = pack2(h[2], h[3]);
            }
            const int tu = 12 * rt + 6 * wm + 2 * i;
            if (colok && tu < 128)
              *(u32x4*)(act + (long)(tu * KT2 + ks) * 1024 + (l31 >> 4) * (KT2 * 1024) + (2 * half + hf) * 256 + l15 * 16) =
                  u32x4{pk[0], pk[1], pk[2], pk[3]};
          }
        }
      } else {
#pragma unroll
        for (int e = 0; e < 2; ++e)
#pragma unroll
          for (int rq = 0; rq < 4; ++rq) {
            const f32x4 c = COLSUM(e, rq);
            if (e == 0 || wn < 3 || rq < 2) {
#pragma unroll
              for (int i = 0; i < 3; ++i) {
                const f32x4 v = (SUB4(acc[i][e], rq) - mean[i] * c) * rstd[i];
                *(u32x2*)(smem + (96 * wm + 32 * i + l31) * QROW + (128 * e + 32 * wn + 8 * rq + 4 * hf) * 2) =
                    u32x2{pack2(v[0], v[1]), pack2(v[2], v[3])};
              }
            }
          }
        __syncthreads();
        const float sc = 0.22360679774997896f * 1.4426950408889634f;
        const bool lo = g4 < 3, hi = g4 < 2;
        const int dq = l15 - 4 * g4;
        const bool par = l15 & 1;
        const int dA = dq - (l15 & 1), dB = dA - 2;
        const bool curA = dA >= 0, curB = dB >= 0;
        const unsigned psh = par ? 16u : 0u;
        const int nit = rt == 11 ? 14 : 22;
#pragma nounroll
        for (int it = w; it < nit; it += 8) {
          const int iu = 1 + (it >> 1);
          const int tu = 11 * rt - 1 + iu;
          const float bA = (curA || tu > 0) ? 0.f : -3.0e38f, bB = (curB || tu > 0) ? 0.f : -3.0e38f;
          const f32x4 z4 = {0.f, 0.f, 0.f, 0.f};
          u32x2 oa[2], ob[2];
#pragma unroll
          for (int e = 0; e < 2; ++e) {
            const int hh = 2 * (it & 1) + e;
            const unsigned rq = sbase + (16 * iu + l15) * QROW + 40 * hh + 16 * g4;
            const u32x2 q0 = lds_read8(rq + 320), q1 = lds_read8(rq + 328);
            const u32x2 a0 = lds_read8(rq - 16 * QROW), a1 = lds_read8(rq - 16 * QROW + 8);
            const u32x2 b0 = lds_read8(rq), b1 = lds_read8(rq + 8);
            const unsigned rv = sbase + (16 * iu + (l >> 2)) * QROW + 160 + 40 * hh + 8 * (l & 3);
            const s16x4 v00 = tr_read(rv - 16 * QROW), v01 = tr_read(rv);
            const s16x4 v10 = tr_read(rv - 16 * QROW + 32), v11 = tr_read(rv + 32);
            const u32x4 qf = {lo ? q0[0] : 0u, lo ? q0[1] : 0u, hi ? q1[0] : 0u, hi ? q1[1] : 0u};
            const u32x4 kp = {a0[0], a0[1], a1[0], a1[1]};
            const u32x4 kc = {b0[0], b0[1], b1[0], b1[1]};
            const f32x4 s0 = MFMA(__builtin_bit_cast(bf16x8, kp), __builtin_bit_cast(bf16x8, qf), z4);
            const f32x4 s1 = MFMA(__builtin_bit_cast(bf16x8, kc), __builtin_bit_cast(bf16x8, qf), z4);
            const float sA = __builtin_fmaf(curA ? (par ? s1[1] : s1[0]) : (par ? s0[1] : s0[0]), sc, bA);
            const float sB = __builtin_fmaf(curB ? (par ? s1[3] : s1[2]) : (par ? s0[3] : s0[2]), sc, bB);
            const float m = xmax4(fmaxf(sA, sB));
            const float eA = __builtin_amdgcn_exp2f(sA - m), eB = __builtin_amdgcn_exp2f(sB - m);
            const float inv = __builtin_amdgcn_rcpf(xsum4(eA + eB));
            const unsigned pk = pack2(eA * inv, eB * inv);
            const unsigned pA = (pk & 0xffffu) << psh, pB = (pk >> 16) << psh;
            const u32x4 pf = {curA ? 0u : pA, curB ? 0u : pB, curA ? pA : 0u, curB ? pB : 0u};
            const f32x4 o0 = MFMA(cat(v00, v01), __builtin_bit_cast(bf16x8, pf), z4);
            const f32x4 o1 = MFMA(cat(v10, v11), __builtin_bit_cast(bf16x8, pf), z4);
            oa[e] = u32x2{pack2(o0[0], o0[1]), pack2(o0[2], o0[3])};
            ob[e] = u32x2{pack2(o1[0], o1[1]), pack2(o1[2], o1[3])};
          }
          {
            char* ab = act + (long)tu * (KT2 * 1024) + l15 * 16;
#pragma unroll
            for (int e = 0; e < 2; ++e) {
              const int c0 = 80 * ct + 40 * (it & 1) + 20 * e;
              const int col = c0 + 4 * g4, col2 = c0 + 16;
              *(u32x2*)(ab + (col >> 3) * 256 + (col & 4) * 2) = oa[e];
              if (g4 == 0) *(u32x2*)(ab + (col2 >> 3) * 256 + (col2 & 4) * 2) = ob[e];
            }
          }
        }
      }
    }
  }

  STAMP(act, 3);
  if (dopack) {
    if (f0 >= 154) PACK_LOAD();
    char* dst = w2p + (long)((4 * pnb + (l15 >> 2)) * KT2 + pks) * 1024 + g4 * 256 + (l15 & 3) * 64;
#pragma unroll
    for (int i = 0; i < 4; ++i) {
      const unsigned sel = (i & 1) ? 0x07060302u : 0x05040100u;
      u32x4 o;
#pragma unroll
      for (int jj = 0; jj < 4; ++jj) o[jj] = __builtin_amdgcn_perm(pv[2 * jj + 1][i >> 1], pv[2 * jj][i >> 1], sel);
      *(u32x4*)(dst + i * 16) = o;
    }
  }
  STAMP(act, 4); STAMPR(act, 9);
}

constexpr int K2D = 6;
__global__ __launch_bounds__(256, 1) void k2_kernel(const char* __restrict__ x, const char* __restrict__ act,
                                                     const char* __restrict__ w2p, char* __restrict__ out) {
  extern __shared__ __attribute__((aligned(16))) char smem[];
  const int tid = threadIdx.x, l = tid & 63;
  const int w = __builtin_amdgcn_readfirstlane(tid >> 6);
  const int b = blockIdx.x, xcd = b & 7, idx = b >> 3;
  const int rt = (xcd >> 1) * 8 + (idx >> 2), ct = (xcd & 1) * 4 + (idx & 3);
  const char* ap = act + (long)(4 * rt * KT2 + w) * 1024;
  const char* bp = w2p + (long)(5 * ct * KT2 + w) * 1024;
  const unsigned lo = l * 16;
  unsigned la[4];
#pragma unroll
  for (int i = 0; i < 4; ++i) la[i] = lo + (i ^ w) * (KT2 * 1024);
  STAMP(act + (1 << 20), 0); STAMPR(act + (1 << 20), 8);
  bf16x8 fa[K2D][4], fb[K2D][5];
  f32x4 acc[4][5];
#define K2_LOAD(d)                                                                                    \
  do {                                                                                                \
    _Pragma("unroll") for (int u = 0; u < 5; ++u) fb[d][u] = *(const bf16x8*)(bp + (lo + u * (KT2 * 1024))); \
    _Pragma("unroll") for (int i = 0; i < 4; ++i) fa[d][i] = *(const bf16x8*)(ap + la[i]); \
    ap += 4096; bp += 4096;                                                                           \
  } while (0)
#define K2_MMA(d)                                                                                     \
  do {                                                                                                \
    _Pragma("unroll") for (int u = 0; u < 5; ++u)                                                     \
    _Pragma("unroll") for (int i = 0; i < 4; ++i)                                                     \
      acc[i][u] = __builtin_amdgcn_mfma_f32_16x16x32_bf16(fb[d][u], fa[d][i], acc[i][u], 0, 0, 0);    \
  } while (0)
#pragma unroll
  for (int d = 0; d < K2D; ++d) K2_LOAD(d);
#pragma unroll
  for (int i = 0; i < 4; ++i)
#pragma unroll
    for (int u = 0; u < 5; ++u) acc[i][u] = f32x4{0.f, 0.f, 0.f, 0.f};
  const int g4 = l >> 4, l15 = l & 15;
  const unsigned ooff = (16 * (4 * rt + w) + l15) * 1280 + (80 * ct + 4 * g4) * 2;
  __builtin_amdgcn_sched_barrier(0);
  STAMP(act + (1 << 20), 1);
#pragma nounroll
  for (int r = 0; r < 18 / K2D - 1; ++r) {
#pragma unroll
    for (int d = 0; d < K2D; ++d) {
      K2_MMA(d);
      K2_LOAD(d);
      __builtin_amdgcn_sched_barrier(0);
    }
  }
  STAMP(act + (1 << 20), 2);
  K2_MMA(0);
  if (w < 2) K2_LOAD(0);
  u32x2 xr[5];
#pragma unroll
  for (int u = 0; u < 5; ++u) xr[u] = *(const u32x2*)(x + (ooff + u * 32));
  __builtin_amdgcn_sched_barrier(0);
#pragma unroll
  for (int d = 1; d < K2D; ++d) K2_MMA(d);
  if (w < 2) K2_MMA(0);
  STAMP(act + (1 << 20), 3);
  f32x4* red = (f32x4*)smem;
#pragma unroll
  for (int i = 1; i < 4; ++i)
#pragma unroll
    for (int u = 0; u < 5; ++u) red[(((i ^ w) * 3 + i - 1) * 5 + u) * 64 + l] = acc[i][u];
  __syncthreads();
#pragma unroll
  for (int u = 0; u < 5; ++u) {
    const f32x4 s = (acc[0][u] + red[((w * 3 + 0) * 5 + u) * 64 + l]) +
                    (red[((w * 3 + 1) * 5 + u) * 64 + l] + red[((w * 3 + 2) * 5 + u) * 64 + l]);
    const float x0 = __builtin_bit_cast(float, xr[u][0] << 16), x1 = __builtin_bit_cast(float, xr[u][0] & 0xffff0000u);
    const float x2 = __builtin_bit_cast(float, xr[u][1] << 16), x3 = __builtin_bit_cast(float, xr[u][1] & 0xffff0000u);
    u32x2 o = {pack2(x0 + s[0], x1 + s[1]), pack2(x2 + s[2], x3 + s[3])};
    *(u32x2*)(out + (ooff + u * 32)) = o;
  }
  STAMP(act + (1 << 20), 4); STAMPR(act + (1 << 20), 9);
}

extern "C" void kernel_launch(void* const* d_in, const int* in_sizes, int n_in, void* d_out, int out_size,
                              void* d_ws, size_t ws_size, hipStream_t stream) {
  const char* x = (const char*)d_in[0];
  const char* wqkv = (const char*)d_in[1];
  const char* wo = (const char*)d_in[2];
  const char* wg = (const char*)d_in[4];
  const char* wu = (const char*)d_in[5];
  const char* wd = (const char*)d_in[6];
  char* act = (char*)d_ws;
  char* w2p = act + (size_t)128 * KT2 * 1024;
  (void)hipFuncSetAttribute((const void*)k1_kernel, hipFuncAttributeMaxDynamicSharedMemorySize, LDS1);
  (void)hipFuncSetAttribute((const void*)k2_kernel, hipFuncAttributeMaxDynamicSharedMemorySize, 61440);
  k1_kernel<<<256, 512, LDS1, stream>>>(x, wqkv, wo, wg, wu, wd, act, w2p);
  k2_kernel<<<256, 256, 61440, stream>>>(x, act, w2p, (char*)d_out);
}
```
